```python
import torch

def _softmax(x, axis=-1):
    return torch.softmax(x, dim=axis)

def _gelu(x):
    return torch.nn.functional.gelu(x, approximate='tanh')

def _silu(x):
    return torch.nn.functional.silu(x)

def _sigmoid(x):
    return torch.sigmoid(x)

def _layernorm(x, axis=-1, eps=1e-05):
    m = x.mean(dim=axis, keepdim=True)
    v = x.var(dim=axis, keepdim=True, unbiased=False)
    return (x - m) / torch.sqrt(v + eps)

def _scatter_add(tgt, idx, src, axis=0):
    return tgt.clone().index_add_(axis, idx, src)

def _segment_sum(src, seg, n):
    out = torch.zeros((n,) + src.shape[1:], dtype=src.dtype)
    return out.index_add_(0, seg, src)

def _pad(x, pw):
    flat = tuple((v for pair in reversed(pw) for v in pair))
    return torch.nn.functional.pad(x, flat)
kernel_name = 'standalone__conformer_family_branchformer__speech'

def setup_inputs():

    _w = lambda a: (lambda t: t.to(torch.bfloat16) if t.is_floating_point() else t)(torch.as_tensor(a))
    B, T, D, K = (2, 4000, 144, 3)
    s = float(D) ** (-0.5)
    return {'x': _w((lambda *s: torch.randn(*s, dtype=torch.bfloat16) * 0.05)(B, T, D)), 'w_q': _w((lambda *s: torch.randn(*s, dtype=torch.bfloat16) * 1.6)(D, D) * s), 'w_k': _w((lambda *s: torch.randn(*s, dtype=torch.bfloat16) * 1.6)(D, D) * s), 'w_v': _w((lambda *s: torch.randn(*s, dtype=torch.bfloat16) * 0.4)(D, D) * s), 'w_ao': _w((lambda *s: torch.randn(*s, dtype=torch.bfloat16) * 0.4)(D, D) * s), 'w_up': _w((lambda *s: torch.randn(*s, dtype=torch.bfloat16) * 0.8)(D, 2 * D) * s), 'dw': _w((lambda *s: torch.randn(*s, dtype=torch.bfloat16) * 0.8)(K, D) * (1.0 / K)), 'w_dn': _w((lambda *s: torch.randn(*s, dtype=torch.bfloat16) * 0.4)(D, D) * s), 'w_merge': _w((lambda *s: torch.randn(*s, dtype=torch.bfloat16) * 0.4)(2 * D, D) * s)}

def reference(x, w_q, w_k, w_v, w_ao, w_up, dw, w_dn, w_merge):
    B, T, D = x.shape
    K = dw.shape[0]
    xa = _layernorm(x, axis=-1)
    q = torch.einsum('btd,de->bte', xa, w_q)
    k = torch.einsum('btd,de->bte', xa, w_k)
    v = torch.einsum('btd,de->bte', xa, w_v)
    logits = torch.einsum('btd,bsd->bts', q, k) * float(D) ** (-0.5)
    attn = _softmax(logits, axis=-1)
    ya = torch.einsum('bts,bsd->btd', attn, v)
    ya = torch.einsum('btd,de->bte', ya, w_ao)
    xc = _layernorm(x, axis=-1)
    h = _gelu(torch.einsum('btd,de->bte', xc, w_up))
    u = h[..., :D].clone()
    g = _layernorm(h[..., D:], axis=-1)
    pad = K // 2
    gp = _pad(g, ((0, 0), (pad, pad), (0, 0)))
    conv = torch.zeros_like(g)
    for i in range(K):
        conv = conv + gp[:, i:i + T, :] * dw[i][None, None, :]
    yc = torch.einsum('btd,de->bte', u * conv, w_dn)
    merged = torch.concatenate([ya, yc], axis=-1)
    out = torch.einsum('btf,fd->btd', merged, w_merge)
    return x + out
```

```cpp
#include <hip/hip_runtime.h>
#include <hip/hip_bf16.h>
typedef unsigned short u16;
constexpr int B = 2, T = 4000, D = 144;
__device__ float b2f(u16 x) { return __uint_as_float((unsigned)x << 16); }
__device__ u16 f2b(float f) { unsigned u = __float_as_uint(f); u += 0x7fff + ((u >> 16) & 1); return u >> 16; }
__global__ __launch_bounds__(192) void k_pre(const u16* x, const u16* wq, const u16* wk, const u16* wv, const u16* wup, float* q, float* k, float* v, float* u, float* g) {
  __shared__ float xa[D], hg[D];
  int row = blockIdx.x, t = threadIdx.x;
  float m = 0;
#pragma unroll 4
 for (int d = 0; d < D; ++d) m += b2f(x[row * D + d]); m /= D;
  float var = 0;
#pragma unroll 4
 for (int d = 0; d < D; ++d) { float e = b2f(x[row * D + d]) - m; var += e * e; } var /= D;
  if (t < D) xa[t] = (b2f(x[row * D + t]) - m) / sqrtf(var + 1e-5f);
  __syncthreads();
  if (t < D) {
    float a = 0, b = 0, c = 0, h1 = 0, h2 = 0;
    for (int d = 0; d < D; ++d) { float z = xa[d]; a += z * b2f(wq[d * D + t]); b += z * b2f(wk[d * D + t]); c += z * b2f(wv[d * D + t]); h1 += z * b2f(wup[d * 2 * D + t]); h2 += z * b2f(wup[d * 2 * D + D + t]); }
    q[row * D + t] = a; k[row * D + t] = b; v[row * D + t] = c;
    auto gelu = [](float z) { return 0.5f * z * (1.f + tanhf(0.7978845608f * (z + 0.044715f * z * z * z))); };
    u[row * D + t] = gelu(h1); hg[t] = gelu(h2);
  }
  __syncthreads();
  if (t < D) { float mm = 0; for (int d = 0; d < D; ++d) mm += hg[d]; mm /= D; float vv = 0; for (int d = 0; d < D; ++d) { float e = hg[d] - mm; vv += e * e; } vv /= D; g[row * D + t] = (hg[t] - mm) / sqrtf(vv + 1e-5f); }
}
__global__ __launch_bounds__(256) void k_attn(const float* q, const float* k, const float* v, float* o) {
  __shared__ float s[T], qs[D], red[256];
  int row = blockIdx.x, b = row / T, t = threadIdx.x;
  if (t < D) qs[t] = q[row * D + t];
  __syncthreads();
  float mx = -1e30f;
  for (int j = t; j < T; j += 256) { float a = 0; const float* kr = k + (b * T + j) * D; for (int d = 0; d < D; ++d) a += qs[d] * kr[d]; a *= (1.f / 12.f); s[j] = a; mx = fmaxf(mx, a); }
  red[t] = mx; __syncthreads();
  for (int i = 128; i; i >>= 1) { if (t < i) red[t] = fmaxf(red[t], red[t + i]); __syncthreads(); }
  mx = red[0]; __syncthreads();
  float l = 0; for (int j = t; j < T; j += 256) { float p = __expf(s[j] - mx); s[j] = p; l += p; }
  red[t] = l; __syncthreads();
  for (int i = 128; i; i >>= 1) { if (t < i) red[t] += red[t + i]; __syncthreads(); }
  l = red[0];
  if (t < D) { float a = 0; for (int j = 0; j < T; ++j) a += s[j] * v[(b * T + j) * D + t]; o[row * D + t] = a / l; }
}
__global__ __launch_bounds__(192) void k_post(const u16* x, const float* o, const float* u, const float* g, const u16* wao, const u16* dw, const u16* wdn, const u16* wm, u16* out) {
  __shared__ float a1[D], a2[D], y[2 * D];
  int row = blockIdx.x, tt = row % T, t = threadIdx.x;
  if (t < D) { a1[t] = o[row * D + t];
    float c = g[row * D + t] * b2f(dw[D + t]); if (tt > 0) c += g[(row - 1) * D + t] * b2f(dw[t]); if (tt < T - 1) c += g[(row + 1) * D + t] * b2f(dw[2 * D + t]);
    a2[t] = u[row * D + t] * c; }
  __syncthreads();
  if (t < D) { float p = 0, r = 0; for (int d = 0; d < D; ++d) { p += a1[d] * b2f(wao[d * D + t]); r += a2[d] * b2f(wdn[d * D + t]); } y[t] = p; y[D + t] = r; }
  __syncthreads();
  if (t < D) { float p = 0; for (int d = 0; d < 2 * D; ++d) p += y[d] * b2f(wm[d * D + t]); out[row * D + t] = f2b(b2f(x[row * D + t]) + p); }
}
extern "C" void kernel_launch(void* const* d_in, const int* in_sizes, int n_in, void* d_out, int out_size, void* d_ws, size_t ws_size, hipStream_t stream) {
  const u16 *x = (const u16*)d_in[0], *wq = (const u16*)d_in[1], *wk = (const u16*)d_in[2], *wv = (const u16*)d_in[3], *wao = (const u16*)d_in[4], *wup = (const u16*)d_in[5], *dw = (const u16*)d_in[6], *wdn = (const u16*)d_in[7], *wm = (const u16*)d_in[8];
  float* w = (float*)d_ws; const int n = B * T * D;
  k_pre<<<B * T, 192, 0, stream>>>(x, wq, wk, wv, wup, w, w + n, w + 2 * n, w + 3 * n, w + 4 * n);
  k_attn<<<B * T, 256, 0, stream>>>(w, w + n, w + 2 * n, w + 5 * n);
  k_post<<<B * T, 192, 0, stream>>>(x, w + 5 * n, w + 3 * n, w + 4 * n, wao, dw, wdn, wm, (u16*)d_out);
}
```

```cpp
#include <hip/hip_runtime.h>
#include <hip/hip_bf16.h>

typedef unsigned short u16;
typedef short s16x4 __attribute__((ext_vector_type(4)));
typedef short s16x8 __attribute__((ext_vector_type(8)));
typedef __bf16 bf16x8 __attribute__((ext_vector_type(8)));
typedef __bf16 bf16x2 __attribute__((ext_vector_type(2)));
typedef float f32x2 __attribute__((ext_vector_type(2)));
typedef float f32x4 __attribute__((ext_vector_type(4)));
typedef unsigned u32x4 __attribute__((ext_vector_type(4)));
#define LDSP __attribute__((address_space(3)))
#define DI __device__ __forceinline__
constexpr unsigned WS_FLAGS = 0x4c0000, WS_G = 0x4c8000, WS_WF = 0x4e0000;

DI unsigned pack2(float lo, float hi) { f32x2 v = {lo, hi}; return __builtin_bit_cast(unsigned, __builtin_convertvector(v, bf16x2)); }
DI void stage(char* dst, const u16* src, int wave, int lane) {
#pragma unroll
  for (int k = 0; k < 11; ++k) {
    const int piece = 4 * k + wave;
    if (k < 10 || piece * 64 + lane < 2592)
      __builtin_amdgcn_global_load_lds((const __attribute__((address_space(1))) void*)((const char*)src + piece * 1024 + lane * 16),
                                       (LDSP void*)(dst + piece * 1024), 16, 0, 0);
  }
}
typedef unsigned u32x2 __attribute__((ext_vector_type(2)));
struct Frag { s16x8 f[5]; };
DI f32x4 mm(s16x8 a, s16x8 b, f32x4 c) {
  return __builtin_amdgcn_mfma_f32_16x16x32_bf16(__builtin_bit_cast(bf16x8, a), __builtin_bit_cast(bf16x8, b), c, 0, 0, 0);
}
DI Frag load_b(const char* b, int colbyte, int lane) {
  const int g = lane >> 4, q = (lane >> 2) & 3;
  Frag r;
#pragma unroll
  for (int s = 0; s < 5; ++s) {
    const int row = 32 * s + 8 * (s < 4 ? g : (g & 1)) + q;
    s16x4 lo = __builtin_amdgcn_ds_read_tr16_b64_v4i16((LDSP s16x4*)(b + row * 288 + colbyte));
    s16x4 hi = __builtin_amdgcn_ds_read_tr16_b64_v4i16((LDSP s16x4*)(b + (row + 4) * 288 + colbyte));
    s16x8 v = __builtin_shufflevector(lo, hi, 0, 1, 2, 3, 4, 5, 6, 7);
    if (s == 4 && g >= 2) v = s16x8{0, 0, 0, 0, 0, 0, 0, 0};
    r.f[s] = v;
  }
  return r;
}
template <bool NAT, bool PERM> DI void rows_mm(const char* a, const Frag& b, f32x4 (&d)[9], int lane) {
  const int g = lane >> 4, i = lane & 15;
#pragma unroll
  for (int tr = 0; tr < 9; ++tr) {
    int row = 16 * tr + i;
    if (PERM && tr < 8) row = (row & ~31) + 16 * ((row >> 2) & 1) + 4 * ((row >> 3) & 3) + (row & 3);
    const char* p = a + row * 288;
    f32x4 acc = {0.f, 0.f, 0.f, 0.f};
#pragma unroll
    for (int s = 0; s < 5; ++s) {
      s16x8 v;
      if (NAT) v = *(const s16x8*)(p + 64 * s + 16 * (s < 4 ? g : (g & 1)));
      else {
        const s16x4 lo = *(const s16x4*)(p + 64 * s + 8 * g), hi = *(const s16x4*)(p + (s < 4 ? 64 * s + 32 : 256) + 8 * g);
        v = __builtin_shufflevector(lo, hi, 0, 1, 2, 3, 4, 5, 6, 7);
      }
      acc = mm(v, b.f[s], acc);
    }
    d[tr] = acc;
  }
}
DI Frag pack(const f32x4 (&d)[9]) {
  Frag r;
#pragma unroll
  for (int s = 0; s < 5; ++s) {
    u32x4 o = {pack2(d[2 * s][0], d[2 * s][1]), pack2(d[2 * s][2], d[2 * s][3]), 0u, 0u};
    if (s < 4) { o[2] = pack2(d[2 * s + 1][0], d[2 * s + 1][1]); o[3] = pack2(d[2 * s + 1][2], d[2 * s + 1][3]); }
    r.f[s] = __builtin_bit_cast(s16x8, o);
  }
  return r;
}
DI float xg_sum(float v) { v += __shfl_xor(v, 16); v += __shfl_xor(v, 32); return v; }
DI int out_cols(int t, int lane) { const int p = lane & 3; return t < 8 ? 2 * (32 * (t >> 1) + 8 * p + 4 * (t & 1)) : 2 * (128 + 4 * p); }
DI void put(char* wf, int t, const Frag& f, int lane) {
  auto rs = __builtin_amdgcn_make_buffer_rsrc(wf, 0, 46080, 0x20000);
#pragma unroll
  for (int s = 0; s < 5; ++s) __builtin_amdgcn_raw_buffer_store_b128(__builtin_bit_cast(u32x4, f.f[s]), rs, ((t * 5 + s) * 64 + lane) * 16, 0, 16);
}

__global__ __launch_bounds__(256, 1) void bf_fused(const u16* x_, const u16* wq_, const u16* wk_, char* ws_, const u16* wup_, const u16* dw_, u16* out_,
                                                   const u16* wv_, const u16* wao_, const u16* wdn_, const u16* wm_) {
  extern __shared__ __attribute__((aligned(16))) char lds[];
  const unsigned b = blockIdx.x;
  const unsigned wave = __builtin_amdgcn_readfirstlane(threadIdx.x >> 6);
  if (!((b >> 3) == 31 && (b & 1))) {
  asm volatile(
"L_start_%=:\n"
"s_load_dwordx16 s[16:31], %0, 0x0\n"
"v_mbcnt_lo_u32_b32 v0, -1, 0\n"
"s_bfe_u32 s74, %1, 0x10002\n"
"v_mbcnt_hi_u32_b32 v0, -1, v0\n"
"s_mov_b32 s77, %2\n"
"s_getpc_b64 s[80:81]\n"
"s_lshl_b32 s50, s77, 11\n"
"v_and_b32 v2, 15, v0\n"
"v_lshlrev_b32 v2, 7, v2\n"
"v_add_u32 v2, s50, v2\n"
"global_load_dword v232, v2, s[80:81]\n"
"s_bfe_u32 s76, %1, 0x10003\n"
"s_lshr_b32 s75, %1, 4\n"
"s_and_b32 s50, %1, 3\n"
"s_lshl2_add_u32 s75, s75, s50\n"
"s_cmp_eq_u32 s75, 63\n"
"s_cselect_b32 s76, 1, s76\n"
"s_cmp_ge_u32 s75, 61\n"
"s_cselect_b32 s50, 1, 0\n"
"s_and_b32 s51, s75, s50\n"
"s_and_b32 s51, s51, 1\n"
"s_cmovk_i32 s75, 63\n"
"s_sub_u32 s75, s75, s50\n"
"s_lshl1_add_u32 s78, s75, s76\n"
"v_and_b32 v21, 15, v0\n"
"v_lshrrev_b32 v22, 4, v0\n"
"s_mov_b32 s38, 1152000\n"
"s_mov_b32 s39, 0x20000\n"
"v_mul_u32_u24 v1, 576, v21\n"
"s_lshr_b32 s50, s77, 1\n"
"s_xor_b32 s50, s50, s78\n"
"s_xor_b32 s50, s50, 1\n"
"s_lshl_b32 s50, s50, 5\n"
"s_and_b32 s51, s77, 1\n"
"s_add_u32 s50, s50, s51\n"
"s_mul_i32 s84, s50, 288\n"
"v_lshl_add_u32 v3, v22, 3, v1\n"
"v_and_b32 v2, 16, v0\n"
"v_lshl_add_u32 v1, v2, 1, v1\n"
"v_lshrrev_b32 v2, 5, v0\n"
"v_lshl_add_u32 v1, v2, 4, v1\n"
"s_getpc_b64 s[80:81]\n"
"L_pc_%=:\n"
"s_movk_i32 s51, 1408\n"
"s_mov_b32 s54, 41468\n"
"s_cmp_eq_u32 s77, 3\n"
"s_cmovk_i32 s51, 2688\n"
"s_cmov_b32 s54, 82940\n"
"s_cmp_eq_u32 s77, 0\n"
"s_cmov_b32 s54, L_end_%=-L_pc_%=-64\n"
"s_lshr_b32 s50, %1, 3\n"
"s_mul_i32 s50, s50, s51\n"
"s_sub_u32 s51, s51, 128\n"
"v_lshlrev_b32 v2, 7, v0\n"
"v_min_u32 v2, s51, v2\n"
"v_add_u32 v2, s50, v2\n"
"v_min_u32 v228, s54, v2\n"
"s_mul_i32 s50, s74, 1152000\n"
"s_waitcnt lgkmcnt(0)\n"
"s_add_u32 s36, s16, s50\n"
"s_addc_u32 s37, s17, 0\n"
"s_and_b32 s37, s37, 0xffff\n"
"buffer_load_dwordx4 v[72:75], v1, s[36:39], s84 offen offset:0\n"
"buffer_load_dwordx4 v[76:79], v1, s[36:39], s84 offen offset:64\n"
"buffer_load_dwordx4 v[80:83], v1, s[36:39], s84 offen offset:128\n"
"buffer_load_dwordx4 v[84:87], v1, s[36:39], s84 offen offset:192\n"
"buffer_load_dwordx2 v[88:89], v3, s[36:39], s84 offen offset:256\n"
"s_mov_b64 s[64:65], s[22:23]\n"
"v_mov_b32 v2, 5013504\n"
"global_load_dword v239, v2, s[64:65] sc1\n"
"s_cmp_eq_u32 s77, 1\n"
"s_cmov_b64 s[80:81], s[18:19]\n"
"s_cmp_eq_u32 s77, 2\n"
"s_cmov_b64 s[80:81], s[20:21]\n"
"s_cmp_eq_u32 s77, 3\n"
"s_cmov_b64 s[80:81], s[24:25]\n"
"global_load_dword v233, v228, s[80:81]\n"
"s_cmp_eq_u32 s77, 2\n"
"s_cbranch_scc0 L_h0_%=\n"
"s_mul_i32 s50, s78, 9216\n"
"s_sub_u32 s50, s50, 288\n"
"v_lshrrev_b32 v23, 5, v0\n"
"v_mul_u32_u24 v23, 9504, v23\n"
"v_and_b32 v64, 31, v0\n"
"v_lshl_add_u32 v23, v64, 4, v23\n"
"v_add_u32 v23, s50, v23\n"
"v_cmp_gt_u32 vcc, 18, v64\n"
"s_nop 1\n"
"v_cndmask_b32 v23, -1, v23, vcc\n"
"buffer_load_dwordx4 v[44:47], v23, s[36:39], 0 offen\n"
"L_h0_%=:\n"
"s_mov_b64 s[56:57], s[18:19]\n"
"s_mov_b32 s56, s56\n"
"s_and_b32 s57, s57, 0xffff\n"
"s_mov_b32 s58, 41472\n"
"s_mov_b32 s59, 0x20000\n"
"v_lshlrev_b32 v10, 4, v0\n"
"s_lshl_b32 s96, s77, 10\n"
"s_mul_i32 s45, s77, 9216\n"
"v_and_b32 v13, 15, v0\n"
"v_lshlrev_b32 v13, 5, v13\n"
"v_and_b32 v14, 16, v0\n"
"v_add_u32 v15, v13, v14\n"
"v_lshrrev_b32 v14, 5, v0\n"
"v_lshl_add_u32 v14, v14, 10, v15\n"
"v_add_u32 v2, s45, v14\n"
"v_bfe_u32 v14, v0, 3, 1\n"
"s_add_u32 m0, s96, 36864\n"
"s_nop 0\n"
"buffer_load_dwordx4 v10, s[56:59], s96 offen lds\n"
"s_addk_i32 s96, 0x1000\n"
"v_lshlrev_b32 v14, 1, v14\n"
"v_lshrrev_b32 v15, 4, v0\n"
"v_xor_b32 v14, v14, v15\n"
"v_lshl_add_u32 v14, v14, 3, v13\n"
"v_add_u32 v14, 0x2000, v14\n"
"v_add_u32 v3, s45, v14\n"
"v_accvgpr_write_b32 a18, 0\n"
"v_accvgpr_write_b32 a19, 0\n"
"s_add_u32 m0, s96, 36864\n"
"s_nop 0\n"
"buffer_load_dwordx4 v10, s[56:59], s96 offen lds\n"
"s_addk_i32 s96, 0x1000\n"
"v_accvgpr_write_b32 a38, 0\n"
"v_accvgpr_write_b32 a39, 0\n"
"v_lshlrev_b32 v13, 3, v0\n"
"v_add_u32 v4, s45, v13\n"
"v_lshrrev_b32 v14, 5, v0\n"
"v_lshlrev_b32 v14, 4, v14\n"
"v_xor_b32 v13, v13, v14\n"
"v_add_u32 v5, s45, v13\n"
"s_add_u32 m0, s96, 36864\n"
"s_nop 0\n"
"buffer_load_dwordx4 v10, s[56:59], s96 offen lds\n"
"s_addk_i32 s96, 0x1000\n"
"v_lshlrev_b32 v23, 3, v0\n"
"v_lshrrev_b32 v68, 2, v0\n"
"v_mul_u32_u24 v68, 288, v68\n"
"v_and_b32 v69, 3, v0\n"
"v_lshl_add_u32 v68, v69, 3, v68\n"
"v_add_u32 v70, 36864, v68\n"
"v_mul_u32_u24 v68, 304, v21\n"
"v_lshl_add_u32 v68, v22, 3, v68\n"
"s_add_u32 m0, s96, 36864\n"
"s_nop 0\n"
"buffer_load_dwordx4 v10, s[56:59], s96 offen lds\n"
"s_addk_i32 s96, 0x1000\n"
"v_add_u32 v71, 78336, v68\n"
"v_add_u32 v6, 73728, v2\n"
"v_add_u32 v7, 73728, v3\n"
"v_add_u32 v8, 73728, v4\n"
"v_add_u32 v9, 73728, v5\n"
"v_mov_b32 v11, v10\n"
"v_mov_b32 v12, v10\n"
"v_mov_b32 v13, v10\n"
"s_add_u32 m0, s96, 36864\n"
"s_nop 0\n"
"buffer_load_dwordx4 v10, s[56:59], s96 offen lds\n"
"s_addk_i32 s96, 0x1000\n"
"v_add_u32 v14, 4096, v10\n"
"v_add_u32 v15, 4096, v10\n"
"v_add_u32 v16, 4096, v10\n"
"v_add_u32 v17, 4096, v10\n"
"v_add_u32 v18, 8192, v10\n"
"v_mov_b64 v[144:145], 0\n"
"v_mov_b64 v[146:147], 0\n"
"v_mov_b64 v[148:149], 0\n"
"s_add_u32 m0, s96, 36864\n"
"s_nop 0\n"
"buffer_load_dwordx4 v10, s[56:59], s96 offen lds\n"
"s_addk_i32 s96, 0x1000\n"
"v_mov_b64 v[150:151], 0\n"
"v_mov_b64 v[152:153], 0\n"
"v_mov_b64 v[154:155], 0\n"
"v_mov_b64 v[156:157], 0\n"
"v_mov_b64 v[158:159], 0\n"
"v_mov_b64 v[160:161], 0\n"
"v_mov_b64 v[162:163], 0\n"
"v_mov_b64 v[164:165], 0\n"
"s_add_u32 m0, s96, 36864\n"
"s_nop 0\n"
"buffer_load_dwordx4 v10, s[56:59], s96 offen lds\n"
"s_addk_i32 s96, 0x1000\n"
"v_mov_b64 v[166:167], 0\n"
"v_mov_b64 v[168:169], 0\n"
"v_mov_b64 v[170:171], 0\n"
"v_mov_b64 v[172:173], 0\n"
"v_mov_b64 v[174:175], 0\n"
"v_mov_b64 v[176:177], 0\n"
"v_mov_b64 v[178:179], 0\n"
"v_mov_b64 v[180:181], 0\n"
"s_add_u32 m0, s96, 36864\n"
"s_nop 0\n"
"buffer_load_dwordx4 v10, s[56:59], s96 offen lds\n"
"s_addk_i32 s96, 0x1000\n"
"v_mov_b64 v[182:183], 0\n"
"v_mov_b64 v[184:185], 0\n"
"v_mov_b64 v[186:187], 0\n"
"v_mov_b64 v[188:189], 0\n"
"v_mov_b64 v[190:191], 0\n"
"v_mov_b64 v[192:193], 0\n"
"v_mov_b64 v[194:195], 0\n"
"v_mov_b64 v[196:197], 0\n"
"s_add_u32 m0, s96, 36864\n"
"s_nop 0\n"
"buffer_load_dwordx4 v10, s[56:59], s96 offen lds\n"
"s_addk_i32 s96, 0x1000\n"
"v_mov_b64 v[198:199], 0\n"
"v_mov_b64 v[200:201], 0\n"
"v_mov_b64 v[202:203], 0\n"
"v_mov_b64 v[204:205], 0\n"
"v_mov_b64 v[206:207], 0\n"
"v_mov_b64 v[208:209], 0\n"
"v_mov_b64 v[210:211], 0\n"
"v_mov_b64 v[212:213], 0\n"
"s_add_u32 m0, s96, 36864\n"
"s_nop 0\n"
"buffer_load_dwordx4 v10, s[56:59], s96 offen lds\n"
"s_addk_i32 s96, 0x1000\n"
"v_mov_b64 v[214:215], 0\n"
"v_accvgpr_write_b32 a94, 0\n"
"v_accvgpr_write_b32 a95, 0\n"
"v_accvgpr_write_b32 a114, 0\n"
"v_accvgpr_write_b32 a115, 0\n"
"v_accvgpr_write_b32 a134, 0\n"
"v_accvgpr_write_b32 a135, 0\n"
"v_accvgpr_write_b32 a154, 0\n"
"v_accvgpr_write_b32 a155, 0\n"
"v_accvgpr_write_b32 a190, 0\n"
"v_accvgpr_write_b32 a191, 0\n"
"v_accvgpr_write_b32 a194, 0\n"
"v_accvgpr_write_b32 a195, 0\n"
"v_mov_b64 v[42:43], 0\n"
"v_mov_b64 v[62:63], 0\n"
"s_mul_i32 s51, s74, 126\n"
"s_add_u32 s54, s78, s51\n"
"s_mul_i32 s55, s54, 9216\n"
"s_add_u32 s72, s64, s55\n"
"s_addc_u32 s73, s65, 0\n"
"s_lshl_b32 s35, s54, 7\n"
"s_and_b32 s50, s77, 1\n"
"s_lshl_b32 s50, s50, 6\n"
"s_add_u32 s35, s35, s50\n"
"s_mul_i32 s50, s54, 10240\n"
"s_mul_i32 s55, s77, 2560\n"
"s_add_u32 s55, s55, 2359296\n"
"s_add_u32 s50, s50, s55\n"
"s_add_u32 s70, s64, s50\n"
"s_addc_u32 s71, s65, 0\n"
"s_lshl2_add_u32 s75, s54, s77\n"
"s_xor_b32 s50, s54, 1\n"
"s_mul_i32 s50, s50, 10240\n"
"s_add_u32 s50, s50, s55\n"
"s_add_u32 s68, s64, s50\n"
"s_addc_u32 s69, s65, 0\n"
"s_mov_b32 s82, 0xffff\n"
"s_add_u32 s50, s77, s76\n"
"s_cmp_ge_u32 s50, 3\n"
"s_cselect_b32 s79, 15, 16\n"
"s_lshl_b32 s50, s77, 4\n"
"s_mul_i32 s55, s76, 63\n"
"s_add_u32 s50, s50, s55\n"
"s_add_u32 s55, s77, s76\n"
"s_cmp_eq_u32 s55, 4\n"
"s_subb_u32 s50, s50, 0\n"
"s_add_u32 s50, s50, s51\n"
"s_mul_i32 s54, s50, 9216\n"
"s_add_u32 s40, s64, s54\n"
"s_addc_u32 s41, s65, 0\n"
"s_and_b32 s41, s41, 0xffff\n"
"s_mul_i32 s42, s79, 9216\n"
"s_mov_b32 s43, 0x20000\n"
"s_add_u32 s66, s64, 4980736\n"
"s_addc_u32 s67, s65, 0\n"
"s_sub_u32 s55, s79, 1\n"
"v_and_b32 v68, 31, v0\n"
"v_min_u32 v19, s55, v68\n"
"v_add_u32 v19, s50, v19\n"
"v_lshlrev_b32 v19, 7, v19\n"
"v_and_b32 v68, 32, v0\n"
"v_lshl_add_u32 v19, v68, 1, v19\n"
"s_lshl_b32 s55, s75, 6\n"
"s_add_u32 s75, s55, 0x10000\n"
"s_xor_b32 s55, s75, 0x100\n"
"v_writelane_b32 v19, s55, 16\n"
"v_writelane_b32 v19, s55, 48\n"
"s_movk_i32 s55, 32256\n"
"v_writelane_b32 v19, s55, 17\n"
"v_writelane_b32 v19, s55, 49\n"
"s_movk_i32 s55, 32384\n"
"v_writelane_b32 v19, s55, 18\n"
"v_writelane_b32 v19, s55, 50\n"
"s_movk_i32 s55, 32512\n"
"v_writelane_b32 v19, s55, 19\n"
"v_writelane_b32 v19, s55, 51\n"
"s_movk_i32 s55, 32640\n"
"v_writelane_b32 v19, s55, 20\n"
"v_writelane_b32 v19, s55, 52\n"
"s_bfm_b32 s47, s79, 0\n"
"s_mov_b32 s53, 0x40000\n"
"s_mov_b32 s46, 4\n"
"s_sub_u32 s50, 16, s79\n"
"s_mul_i32 s85, s50, 73728\n"
"s_add_u32 s85, s85, s45\n"
"v_mov_b32 v238, v10\n"
"v_mov_b32 v236, 0xc0135761\n"
"v_mov_b32 v234, 1.0\n"
"s_mov_b32 s92, 0xbdd2d3e8\n"
"s_mov_b32 s93, s92\n"
"v_lshrrev_b32 v56, 2, v0\n"
"v_and_b32 v57, 3, v0\n"
"v_bfe_u32 v58, v0, 4, 1\n"
"v_lshlrev_b32 v250, 6, v56\n"
"v_lshl_add_u32 v250, v57, 3, v250\n"
"v_lshl_add_u32 v250, v58, 5, v250\n"
"v_xor_b32 v251, 32, v250\n"
"v_mul_u32_u24 v56, 576, v56\n"
"v_lshlrev_b32 v58, 1, v58\n"
"v_xor_b32 v57, v57, v58\n"
"v_lshl_add_u32 v252, v57, 4, v56\n"
"v_lshrrev_b32 v57, 1, v57\n"
"v_lshl_add_u32 v235, v57, 8, v252\n"
"v_add_u32 v235, 0x100, v235\n"
"s_mul_i32 s50, s74, 1152000\n"
"s_add_u32 s60, s28, s50\n"
"s_addc_u32 s61, s29, 0\n"
"s_and_b32 s61, s61, 0xffff\n"
"s_mov_b32 s28, s24\n"
"s_and_b32 s29, s25, 0xffff\n"
"s_mov_b32 s30, 82944\n"
"s_mov_b32 s31, 0x20000\n"
"s_mov_b32 s24, s26\n"
"s_and_b32 s25, s27, 0xffff\n"
"s_mov_b32 s26, 864\n"
"s_mov_b32 s27, 0x20000\n"
"s_cmp_eq_u32 s77, 1\n"
"s_cbranch_scc0 L_wq1_%=\n"
"s_mov_b32 m0, 77824\n"
"s_mov_b32 s96, 40960\n"
"s_mov_b32 exec_hi, 0\n"
"buffer_load_dwordx4 v10, s[56:59], s96 offen lds\n"
"s_mov_b32 exec_hi, -1\n"
"L_wq1_%=:\n"
"s_mov_b64 s[56:57], s[20:21]\n"
"s_mov_b32 s56, s56\n"
"s_and_b32 s57, s57, 0xffff\n"
"s_mov_b32 s58, 41472\n"
"s_mov_b32 s59, 0x20000\n"
"s_lshl_b32 s50, s77, 6\n"
"v_add_u32 v60, s50, v0\n"
"s_lshl_b32 s96, s77, 10\n"
"s_add_u32 s96, s96, 78336\n"
"s_mov_b32 s55, 0xffff0000\n"
"s_cmp_ge_u32 s77, 2\n"
"s_cbranch_scc1 L_zp_%=\n"
"v_mul_u32_u24 v61, 3450, v60\n"
"s_mov_b32 m0, s96\n"
"v_lshrrev_b32 v61, 16, v61\n"
"v_sub_u32 v61, v60, v61\n"
"v_lshlrev_b32 v61, 4, v61\n"
"buffer_load_dwordx4 v61, s[56:59], 0 offen lds\n"
"v_add_u32 v60, 0x80, v60\n"
"s_addk_i32 s96, 0x800\n"
"v_mul_u32_u24 v61, 3450, v60\n"
"s_mov_b32 m0, s96\n"
"v_lshrrev_b32 v61, 16, v61\n"
"v_sub_u32 v61, v60, v61\n"
"v_lshlrev_b32 v61, 4, v61\n"
"buffer_load_dwordx4 v61, s[56:59], 0 offen lds\n"
"v_add_u32 v60, 0x80, v60\n"
"s_addk_i32 s96, 0x800\n"
"v_mul_u32_u24 v61, 3450, v60\n"
"s_mov_b32 m0, s96\n"
"v_lshrrev_b32 v61, 16, v61\n"
"v_sub_u32 v61, v60, v61\n"
"v_lshlrev_b32 v61, 4, v61\n"
"buffer_load_dwordx4 v61, s[56:59], 0 offen lds\n"
"v_add_u32 v60, 0x80, v60\n"
"s_addk_i32 s96, 0x800\n"
"v_mul_u32_u24 v61, 3450, v60\n"
"s_mov_b32 m0, s96\n"
"v_lshrrev_b32 v61, 16, v61\n"
"v_sub_u32 v61, v60, v61\n"
"v_lshlrev_b32 v61, 4, v61\n"
"buffer_load_dwordx4 v61, s[56:59], 0 offen lds\n"
"v_add_u32 v60, 0x80, v60\n"
"s_addk_i32 s96, 0x800\n"
"v_mul_u32_u24 v61, 3450, v60\n"
"s_mov_b32 m0, s96\n"
"v_lshrrev_b32 v61, 16, v61\n"
"v_sub_u32 v61, v60, v61\n"
"v_lshlrev_b32 v61, 4, v61\n"
"buffer_load_dwordx4 v61, s[56:59], 0 offen lds\n"
"v_add_u32 v60, 0x80, v60\n"
"s_addk_i32 s96, 0x800\n"
"v_mul_u32_u24 v61, 3450, v60\n"
"s_mov_b32 m0, s96\n"
"v_lshrrev_b32 v61, 16, v61\n"
"v_sub_u32 v61, v60, v61\n"
"v_lshlrev_b32 v61, 4, v61\n"
"buffer_load_dwordx4 v61, s[56:59], 0 offen lds\n"
"v_add_u32 v60, 0x80, v60\n"
"s_addk_i32 s96, 0x800\n"
"v_mul_u32_u24 v61, 3450, v60\n"
"s_mov_b32 m0, s96\n"
"v_lshrrev_b32 v61, 16, v61\n"
"v_sub_u32 v61, v60, v61\n"
"v_lshlrev_b32 v61, 4, v61\n"
"buffer_load_dwordx4 v61, s[56:59], 0 offen lds\n"
"v_add_u32 v60, 0x80, v60\n"
"s_addk_i32 s96, 0x800\n"
"v_mul_u32_u24 v61, 3450, v60\n"
"s_mov_b32 m0, s96\n"
"v_lshrrev_b32 v61, 16, v61\n"
"v_sub_u32 v61, v60, v61\n"
"v_lshlrev_b32 v61, 4, v61\n"
"buffer_load_dwordx4 v61, s[56:59], 0 offen lds\n"
"v_add_u32 v60, 0x80, v60\n"
"s_addk_i32 s96, 0x800\n"
"v_mul_u32_u24 v61, 3450, v60\n"
"s_mov_b32 m0, s96\n"
"v_lshrrev_b32 v61, 16, v61\n"
"v_sub_u32 v61, v60, v61\n"
"v_lshlrev_b32 v61, 4, v61\n"
"buffer_load_dwordx4 v61, s[56:59], 0 offen lds\n"
"v_add_u32 v60, 0x80, v60\n"
"s_addk_i32 s96, 0x800\n"
"v_mul_u32_u24 v61, 3450, v60\n"
"s_mov_b32 m0, s96\n"
"v_lshrrev_b32 v61, 16, v61\n"
"v_sub_u32 v61, v60, v61\n"
"v_lshlrev_b32 v61, 4, v61\n"
"buffer_load_dwordx4 v61, s[56:59], 0 offen lds\n"
"v_add_u32 v60, 0x80, v60\n"
"s_addk_i32 s96, 0x800\n"
"v_mul_u32_u24 v61, 3450, v60\n"
"s_mov_b32 m0, s96\n"
"v_lshrrev_b32 v61, 16, v61\n"
"v_sub_u32 v61, v60, v61\n"
"v_lshlrev_b32 v61, 4, v61\n"
"buffer_load_dwordx4 v61, s[56:59], 0 offen lds\n"
"v_add_u32 v60, 0x80, v60\n"
"s_addk_i32 s96, 0x800\n"
"v_mul_u32_u24 v61, 3450, v60\n"
"s_mov_b32 m0, s96\n"
"v_lshrrev_b32 v61, 16, v61\n"
"v_sub_u32 v61, v60, v61\n"
"v_lshlrev_b32 v61, 4, v61\n"
"buffer_load_dwordx4 v61, s[56:59], 0 offen lds\n"
"v_add_u32 v60, 0x80, v60\n"
"s_addk_i32 s96, 0x800\n"
"v_mul_u32_u24 v61, 3450, v60\n"
"s_mov_b32 m0, s96\n"
"v_lshrrev_b32 v61, 16, v61\n"
"v_sub_u32 v61, v60, v61\n"
"v_lshlrev_b32 v61, 4, v61\n"
"buffer_load_dwordx4 v61, s[56:59], 0 offen lds\n"
"v_add_u32 v60, 0x80, v60\n"
"s_addk_i32 s96, 0x800\n"
"v_mul_u32_u24 v61, 3450, v60\n"
"s_mov_b32 m0, s96\n"
"v_lshrrev_b32 v61, 16, v61\n"
"v_sub_u32 v61, v60, v61\n"
"v_lshlrev_b32 v61, 4, v61\n"
"buffer_load_dwordx4 v61, s[56:59], 0 offen lds\n"
"v_add_u32 v60, 0x80, v60\n"
"s_addk_i32 s96, 0x800\n"
"v_mul_u32_u24 v61, 3450, v60\n"
"s_mov_b32 m0, s96\n"
"v_lshrrev_b32 v61, 16, v61\n"
"v_sub_u32 v61, v60, v61\n"
"v_lshlrev_b32 v61, 4, v61\n"
"buffer_load_dwordx4 v61, s[56:59], 0 offen lds\n"
"v_add_u32 v60, 0x80, v60\n"
"s_addk_i32 s96, 0x800\n"
"v_mul_u32_u24 v61, 3450, v60\n"
"s_mov_b32 m0, s96\n"
"v_lshrrev_b32 v61, 16, v61\n"
"v_sub_u32 v61, v60, v61\n"
"v_lshlrev_b32 v61, 4, v61\n"
"buffer_load_dwordx4 v61, s[56:59], 0 offen lds\n"
"v_add_u32 v60, 0x80, v60\n"
"s_addk_i32 s96, 0x800\n"
"v_mul_u32_u24 v61, 3450, v60\n"
"s_mov_b32 m0, s96\n"
"v_lshrrev_b32 v61, 16, v61\n"
"v_sub_u32 v61, v60, v61\n"
"v_lshlrev_b32 v61, 4, v61\n"
"buffer_load_dwordx4 v61, s[56:59], 0 offen lds\n"
"v_add_u32 v60, 0x80, v60\n"
"s_addk_i32 s96, 0x800\n"
"v_mul_u32_u24 v61, 3450, v60\n"
"s_mov_b32 m0, s96\n"
"v_lshrrev_b32 v61, 16, v61\n"
"v_sub_u32 v61, v60, v61\n"
"v_lshlrev_b32 v61, 4, v61\n"
"buffer_load_dwordx4 v61, s[56:59], 0 offen lds\n"
"v_add_u32 v60, 0x80, v60\n"
"s_addk_i32 s96, 0x800\n"
"v_mul_u32_u24 v61, 3450, v60\n"
"s_mov_b32 m0, s96\n"
"v_lshrrev_b32 v61, 16, v61\n"
"v_sub_u32 v61, v60, v61\n"
"v_lshlrev_b32 v61, 4, v61\n"
"buffer_load_dwordx4 v61, s[56:59], 0 offen lds\n"
"v_add_u32 v60, 0x80, v60\n"
"s_addk_i32 s96, 0x800\n"
"v_mul_u32_u24 v61, 3450, v60\n"
"s_mov_b32 m0, s96\n"
"v_lshrrev_b32 v61, 16, v61\n"
"v_sub_u32 v61, v60, v61\n"
"v_lshlrev_b32 v61, 4, v61\n"
"buffer_load_dwordx4 v61, s[56:59], 0 offen lds\n"
"v_add_u32 v60, 0x80, v60\n"
"s_addk_i32 s96, 0x800\n"
"v_mul_u32_u24 v61, 3450, v60\n"
"s_mov_b32 m0, s96\n"
"v_lshrrev_b32 v61, 16, v61\n"
"v_sub_u32 v61, v60, v61\n"
"v_lshlrev_b32 v61, 4, v61\n"
"buffer_load_dwordx4 v61, s[56:59], 0 offen lds\n"
"v_add_u32 v60, 0x80, v60\n"
"s_addk_i32 s96, 0x800\n"
"s_cmp_eq_u32 s77, 0\n"
"s_cbranch_scc0 L_k0_%=\n"
"v_mul_u32_u24 v61, 3450, v60\n"
"s_mov_b32 m0, s96\n"
"v_lshrrev_b32 v61, 16, v61\n"
"v_sub_u32 v61, v60, v61\n"
"v_lshlrev_b32 v61, 4, v61\n"
"buffer_load_dwordx4 v61, s[56:59], 0 offen lds\n"
"v_add_u32 v60, 0x80, v60\n"
"s_addk_i32 s96, 0x800\n"
"L_k0_%=:\n"
"s_waitcnt vmcnt(37)\n"
"v_permlane16_swap_b32 v72, v74\n"
"v_permlane16_swap_b32 v73, v75\n"
"s_waitcnt vmcnt(36)\n"
"v_permlane16_swap_b32 v76, v78\n"
"v_permlane16_swap_b32 v77, v79\n"
"s_waitcnt vmcnt(35)\n"
"v_permlane16_swap_b32 v80, v82\n"
"v_permlane16_swap_b32 v81, v83\n"
"s_waitcnt vmcnt(34)\n"
"v_permlane16_swap_b32 v84, v86\n"
"v_permlane16_swap_b32 v85, v87\n"
"v_lshlrev_b32 v90, 16, v72\n"
"v_and_b32 v91, s55, v72\n"
"v_lshlrev_b32 v92, 16, v73\n"
"v_and_b32 v93, s55, v73\n"
"v_lshlrev_b32 v94, 16, v74\n"
"v_and_b32 v95, s55, v74\n"
"v_lshlrev_b32 v96, 16, v75\n"
"v_and_b32 v97, s55, v75\n"
"v_lshlrev_b32 v98, 16, v76\n"
"v_and_b32 v99, s55, v76\n"
"v_lshlrev_b32 v100, 16, v77\n"
"v_and_b32 v101, s55, v77\n"
"v_lshlrev_b32 v102, 16, v78\n"
"v_and_b32 v103, s55, v78\n"
"v_lshlrev_b32 v104, 16, v79\n"
"v_and_b32 v105, s55, v79\n"
"v_lshlrev_b32 v106, 16, v80\n"
"v_and_b32 v107, s55, v80\n"
"v_lshlrev_b32 v108, 16, v81\n"
"v_and_b32 v109, s55, v81\n"
"v_lshlrev_b32 v110, 16, v82\n"
"v_and_b32 v111, s55, v82\n"
"v_lshlrev_b32 v112, 16, v83\n"
"v_and_b32 v113, s55, v83\n"
"v_lshlrev_b32 v114, 16, v84\n"
"v_and_b32 v115, s55, v84\n"
"v_lshlrev_b32 v116, 16, v85\n"
"v_and_b32 v117, s55, v85\n"
"v_lshlrev_b32 v118, 16, v86\n"
"v_and_b32 v119, s55, v86\n"
"v_lshlrev_b32 v120, 16, v87\n"
"v_and_b32 v121, s55, v87\n"
"s_waitcnt vmcnt(33)\n"
"v_lshlrev_b32 v122, 16, v88\n"
"v_and_b32 v123, s55, v88\n"
"v_lshlrev_b32 v124, 16, v89\n"
"v_and_b32 v125, s55, v89\n"
"v_pk_add_f32 v[64:65], v[90:91], v[92:93]\n"
"v_pk_mul_f32 v[66:67], v[90:91], v[90:91]\n"
"v_pk_fma_f32 v[66:67], v[92:93], v[92:93], v[66:67]\n"
"v_pk_add_f32 v[64:65], v[64:65], v[94:95]\n"
"v_pk_fma_f32 v[66:67], v[94:95], v[94:95], v[66:67]\n"
"v_pk_add_f32 v[64:65], v[64:65], v[96:97]\n"
"v_pk_fma_f32 v[66:67], v[96:97], v[96:97], v[66:67]\n"
"v_pk_add_f32 v[64:65], v[64:65], v[98:99]\n"
"v_pk_fma_f32 v[66:67], v[98:99], v[98:99], v[66:67]\n"
"v_pk_add_f32 v[64:65], v[64:65], v[100:101]\n"
"v_pk_fma_f32 v[66:67], v[100:101], v[100:101], v[66:67]\n"
"v_pk_add_f32 v[64:65], v[64:65], v[102:103]\n"
"v_pk_fma_f32 v[66:67], v[102:103], v[102:103], v[66:67]\n"
"v_pk_add_f32 v[64:65], v[64:65], v[104:105]\n"
"v_pk_fma_f32 v[66:67], v[104:105], v[104:105], v[66:67]\n"
"v_pk_add_f32 v[64:65], v[64:65], v[106:107]\n"
"v_pk_fma_f32 v[66:67], v[106:107], v[106:107], v[66:67]\n"
"v_pk_add_f32 v[64:65], v[64:65], v[108:109]\n"
"v_pk_fma_f32 v[66:67], v[108:109], v[108:109], v[66:67]\n"
"v_pk_add_f32 v[64:65], v[64:65], v[110:111]\n"
"v_pk_fma_f32 v[66:67], v[110:111], v[110:111], v[66:67]\n"
"v_pk_add_f32 v[64:65], v[64:65], v[112:113]\n"
"v_pk_fma_f32 v[66:67], v[112:113], v[112:113], v[66:67]\n"
"v_pk_add_f32 v[64:65], v[64:65], v[114:115]\n"
"v_pk_fma_f32 v[66:67], v[114:115], v[114:115], v[66:67]\n"
"v_pk_add_f32 v[64:65], v[64:65], v[116:117]\n"
"v_pk_fma_f32 v[66:67], v[116:117], v[116:117], v[66:67]\n"
"v_pk_add_f32 v[64:65], v[64:65], v[118:119]\n"
"v_pk_fma_f32 v[66:67], v[118:119], v[118:119], v[66:67]\n"
"v_pk_add_f32 v[64:65], v[64:65], v[120:121]\n"
"v_pk_fma_f32 v[66:67], v[120:121], v[120:121], v[66:67]\n"
"v_pk_add_f32 v[64:65], v[64:65], v[122:123]\n"
"v_pk_fma_f32 v[66:67], v[122:123], v[122:123], v[66:67]\n"
"v_pk_add_f32 v[64:65], v[64:65], v[124:125]\n"
"v_pk_fma_f32 v[66:67], v[124:125], v[124:125], v[66:67]\n"
"v_cmp_le_u32 vcc, 48, v0\n"
"v_add_f32 v64, v64, v65\n"
"v_add_f32 v66, v66, v67\n"
"v_cndmask_b32 v65, 0, v125, vcc\n"
"v_mfma_f32_16x16x4_f32 v[24:27], v234, v64, 0\n"
"v_mfma_f32_16x16x4_f32 v[28:31], v234, v66, 0\n"
"v_mfma_f32_16x16x4_f32 v[32:35], v234, v65, 0\n"
"v_mul_f32 v64, 0x3be38e39, v24\n"
"s_nop 5\n"
"v_mul_f32 v66, 0x3be38e39, v28\n"
"s_nop 3\n"
"v_mov_b32 v65, v32\n"
"v_fma_f32 v66, -v64, v64, v66\n"
"v_max_f32 v66, 0, v66\n"
"v_add_f32 v66, 0x3727c5ac, v66\n"
"v_rsq_f32 v66, v66\n"
"s_nop 0\n"
"v_mul_f32 v64, v64, v66\n"
"v_fma_f32 v65, v65, v66, -v64\n"
"v_sub_f32 v68, 0, v64\n"
"v_mul_f32 v65, 0x3dba2e8c, v65\n"
"v_sub_f32 v64, v65, v64\n"
"v_pk_fma_f32 v[72:73], v[90:91], v[66:67], v[68:69] op_sel_hi:[1,0,0]\n"
"v_pk_fma_f32 v[74:75], v[92:93], v[66:67], v[68:69] op_sel_hi:[1,0,0]\n"
"v_pk_fma_f32 v[76:77], v[94:95], v[66:67], v[68:69] op_sel_hi:[1,0,0]\n"
"v_pk_fma_f32 v[78:79], v[96:97], v[66:67], v[68:69] op_sel_hi:[1,0,0]\n"
"v_cvt_pk_bf16_f32 v24, v72, v73\n"
"v_cvt_pk_bf16_f32 v25, v74, v75\n"
"v_cvt_pk_bf16_f32 v26, v76, v77\n"
"v_cvt_pk_bf16_f32 v27, v78, v79\n"
"v_pk_fma_f32 v[72:73], v[98:99], v[66:67], v[68:69] op_sel_hi:[1,0,0]\n"
"v_pk_fma_f32 v[74:75], v[100:101], v[66:67], v[68:69] op_sel_hi:[1,0,0]\n"
"v_pk_fma_f32 v[76:77], v[102:103], v[66:67], v[68:69] op_sel_hi:[1,0,0]\n"
"v_pk_fma_f32 v[78:79], v[104:105], v[66:67], v[68:69] op_sel_hi:[1,0,0]\n"
"v_cvt_pk_bf16_f32 v28, v72, v73\n"
"v_cvt_pk_bf16_f32 v29, v74, v75\n"
"v_cvt_pk_bf16_f32 v30, v76, v77\n"
"v_cvt_pk_bf16_f32 v31, v78, v79\n"
"v_pk_fma_f32 v[72:73], v[106:107], v[66:67], v[68:69] op_sel_hi:[1,0,0]\n"
"v_pk_fma_f32 v[74:75], v[108:109], v[66:67], v[68:69] op_sel_hi:[1,0,0]\n"
"v_pk_fma_f32 v[76:77], v[110:111], v[66:67], v[68:69] op_sel_hi:[1,0,0]\n"
"v_pk_fma_f32 v[78:79], v[112:113], v[66:67], v[68:69] op_sel_hi:[1,0,0]\n"
"v_cvt_pk_bf16_f32 v32, v72, v73\n"
"v_cvt_pk_bf16_f32 v33, v74, v75\n"
"v_cvt_pk_bf16_f32 v34, v76, v77\n"
"v_cvt_pk_bf16_f32 v35, v78, v79\n"
"v_pk_fma_f32 v[72:73], v[114:115], v[66:67], v[68:69] op_sel_hi:[1,0,0]\n"
"v_pk_fma_f32 v[74:75], v[116:117], v[66:67], v[68:69] op_sel_hi:[1,0,0]\n"
"v_pk_fma_f32 v[76:77], v[118:119], v[66:67], v[68:69] op_sel_hi:[1,0,0]\n"
"v_pk_fma_f32 v[78:79], v[120:121], v[66:67], v[68:69] op_sel_hi:[1,0,0]\n"
"v_cvt_pk_bf16_f32 v36, v72, v73\n"
"v_cvt_pk_bf16_f32 v37, v74, v75\n"
"v_cvt_pk_bf16_f32 v38, v76, v77\n"
"v_cvt_pk_bf16_f32 v39, v78, v79\n"
"v_pk_fma_f32 v[72:73], v[122:123], v[66:67], v[68:69] op_sel_hi:[1,0,0]\n"
"v_pk_fma_f32 v[74:75], v[124:125], v[66:67], v[68:69] op_sel_hi:[1,0,0]\n"
"v_cvt_pk_bf16_f32 v40, v72, v73\n"
"v_cvt_pk_bf16_f32 v41, v74, v75\n"
"s_waitcnt vmcnt(21)\n"
"v_readfirstlane_b32 s49, v239\n"
"s_branch L_zj_%=\n"
"L_zp_%=:\n"
"s_waitcnt vmcnt(16)\n"
"s_nop 0\n"
"v_permlane16_swap_b32 v72, v74\n"
"v_permlane16_swap_b32 v73, v75\n"
"s_waitcnt vmcnt(15)\n"
"v_permlane16_swap_b32 v76, v78\n"
"v_permlane16_swap_b32 v77, v79\n"
"s_waitcnt vmcnt(14)\n"
"v_permlane16_swap_b32 v80, v82\n"
"v_permlane16_swap_b32 v81, v83\n"
"s_waitcnt vmcnt(13)\n"
"v_permlane16_swap_b32 v84, v86\n"
"v_permlane16_swap_b32 v85, v87\n"
"v_lshlrev_b32 v90, 16, v72\n"
"v_and_b32 v91, s55, v72\n"
"v_lshlrev_b32 v92, 16, v73\n"
"v_and_b32 v93, s55, v73\n"
"v_lshlrev_b32 v94, 16, v74\n"
"v_and_b32 v95, s55, v74\n"
"v_lshlrev_b32 v96, 16, v75\n"
"v_and_b32 v97, s55, v75\n"
"v_lshlrev_b32 v98, 16, v76\n"
"v_and_b32 v99, s55, v76\n"
"v_lshlrev_b32 v100, 16, v77\n"
"v_and_b32 v101, s55, v77\n"
"v_lshlrev_b32 v102, 16, v78\n"
"v_and_b32 v103, s55, v78\n"
"v_lshlrev_b32 v104, 16, v79\n"
"v_and_b32 v105, s55, v79\n"
"v_lshlrev_b32 v106, 16, v80\n"
"v_and_b32 v107, s55, v80\n"
"v_lshlrev_b32 v108, 16, v81\n"
"v_and_b32 v109, s55, v81\n"
"v_lshlrev_b32 v110, 16, v82\n"
"v_and_b32 v111, s55, v82\n"
"v_lshlrev_b32 v112, 16, v83\n"
"v_and_b32 v113, s55, v83\n"
"v_lshlrev_b32 v114, 16, v84\n"
"v_and_b32 v115, s55, v84\n"
"v_lshlrev_b32 v116, 16, v85\n"
"v_and_b32 v117, s55, v85\n"
"v_lshlrev_b32 v118, 16, v86\n"
"v_and_b32 v119, s55, v86\n"
"v_lshlrev_b32 v120, 16, v87\n"
"v_and_b32 v121, s55, v87\n"
"s_waitcnt vmcnt(12)\n"
"v_lshlrev_b32 v122, 16, v88\n"
"v_and_b32 v123, s55, v88\n"
"v_lshlrev_b32 v124, 16, v89\n"
"v_and_b32 v125, s55, v89\n"
"v_pk_add_f32 v[64:65], v[90:91], v[92:93]\n"
"v_pk_mul_f32 v[66:67], v[90:91], v[90:91]\n"
"v_pk_fma_f32 v[66:67], v[92:93], v[92:93], v[66:67]\n"
"v_pk_add_f32 v[64:65], v[64:65], v[94:95]\n"
"v_pk_fma_f32 v[66:67], v[94:95], v[94:95], v[66:67]\n"
"v_pk_add_f32 v[64:65], v[64:65], v[96:97]\n"
"v_pk_fma_f32 v[66:67], v[96:97], v[96:97], v[66:67]\n"
"v_pk_add_f32 v[64:65], v[64:65], v[98:99]\n"
"v_pk_fma_f32 v[66:67], v[98:99], v[98:99], v[66:67]\n"
"v_pk_add_f32 v[64:65], v[64:65], v[100:101]\n"
"v_pk_fma_f32 v[66:67], v[100:101], v[100:101], v[66:67]\n"
"v_pk_add_f32 v[64:65], v[64:65], v[102:103]\n"
"v_pk_fma_f32 v[66:67], v[102:103], v[102:103], v[66:67]\n"
"v_pk_add_f32 v[64:65], v[64:65], v[104:105]\n"
"v_pk_fma_f32 v[66:67], v[104:105], v[104:105], v[66:67]\n"
"v_pk_add_f32 v[64:65], v[64:65], v[106:107]\n"
"v_pk_fma_f32 v[66:67], v[106:107], v[106:107], v[66:67]\n"
"v_pk_add_f32 v[64:65], v[64:65], v[108:109]\n"
"v_pk_fma_f32 v[66:67], v[108:109], v[108:109], v[66:67]\n"
"v_pk_add_f32 v[64:65], v[64:65], v[110:111]\n"
"v_pk_fma_f32 v[66:67], v[110:111], v[110:111], v[66:67]\n"
"v_pk_add_f32 v[64:65], v[64:65], v[112:113]\n"
"v_pk_fma_f32 v[66:67], v[112:113], v[112:113], v[66:67]\n"
"v_pk_add_f32 v[64:65], v[64:65], v[114:115]\n"
"v_pk_fma_f32 v[66:67], v[114:115], v[114:115], v[66:67]\n"
"v_pk_add_f32 v[64:65], v[64:65], v[116:117]\n"
"v_pk_fma_f32 v[66:67], v[116:117], v[116:117], v[66:67]\n"
"v_pk_add_f32 v[64:65], v[64:65], v[118:119]\n"
"v_pk_fma_f32 v[66:67], v[118:119], v[118:119], v[66:67]\n"
"v_pk_add_f32 v[64:65], v[64:65], v[120:121]\n"
"v_pk_fma_f32 v[66:67], v[120:121], v[120:121], v[66:67]\n"
"v_pk_add_f32 v[64:65], v[64:65], v[122:123]\n"
"v_pk_fma_f32 v[66:67], v[122:123], v[122:123], v[66:67]\n"
"v_pk_add_f32 v[64:65], v[64:65], v[124:125]\n"
"v_pk_fma_f32 v[66:67], v[124:125], v[124:125], v[66:67]\n"
"v_cmp_le_u32 vcc, 48, v0\n"
"v_add_f32 v64, v64, v65\n"
"v_add_f32 v66, v66, v67\n"
"v_cndmask_b32 v65, 0, v125, vcc\n"
"v_mfma_f32_16x16x4_f32 v[24:27], v234, v64, 0\n"
"v_mfma_f32_16x16x4_f32 v[28:31], v234, v66, 0\n"
"v_mfma_f32_16x16x4_f32 v[32:35], v234, v65, 0\n"
"v_mul_f32 v64, 0x3be38e39, v24\n"
"s_nop 5\n"
"v_mul_f32 v66, 0x3be38e39, v28\n"
"s_nop 3\n"
"v_mov_b32 v65, v32\n"
"v_fma_f32 v66, -v64, v64, v66\n"
"v_max_f32 v66, 0, v66\n"
"v_add_f32 v66, 0x3727c5ac, v66\n"
"v_rsq_f32 v66, v66\n"
"s_nop 0\n"
"v_mul_f32 v64, v64, v66\n"
"v_fma_f32 v65, v65, v66, -v64\n"
"v_sub_f32 v68, 0, v64\n"
"v_mul_f32 v65, 0x3dba2e8c, v65\n"
"v_sub_f32 v64, v65, v64\n"
"v_pk_fma_f32 v[72:73], v[90:91], v[66:67], v[64:65] op_sel_hi:[1,0,0]\n"
"v_pk_fma_f32 v[74:75], v[92:93], v[66:67], v[64:65] op_sel_hi:[1,0,0]\n"
"v_pk_fma_f32 v[76:77], v[94:95], v[66:67], v[64:65] op_sel_hi:[1,0,0]\n"
"v_pk_fma_f32 v[78:79], v[96:97], v[66:67], v[64:65] op_sel_hi:[1,0,0]\n"
"v_cvt_pk_bf16_f32 v126, v72, v73\n"
"v_cvt_pk_bf16_f32 v127, v74, v75\n"
"v_cvt_pk_bf16_f32 v128, v76, v77\n"
"v_cvt_pk_bf16_f32 v129, v78, v79\n"
"v_pk_fma_f32 v[72:73], v[98:99], v[66:67], v[64:65] op_sel_hi:[1,0,0]\n"
"v_pk_fma_f32 v[74:75], v[100:101], v[66:67], v[64:65] op_sel_hi:[1,0,0]\n"
"v_pk_fma_f32 v[76:77], v[102:103], v[66:67], v[64:65] op_sel_hi:[1,0,0]\n"
"v_pk_fma_f32 v[78:79], v[104:105], v[66:67], v[64:65] op_sel_hi:[1,0,0]\n"
"v_cvt_pk_bf16_f32 v130, v72, v73\n"
"v_cvt_pk_bf16_f32 v131, v74, v75\n"
"v_cvt_pk_bf16_f32 v132, v76, v77\n"
"v_cvt_pk_bf16_f32 v133, v78, v79\n"
"v_pk_fma_f32 v[72:73], v[106:107], v[66:67], v[64:65] op_sel_hi:[1,0,0]\n"
"v_pk_fma_f32 v[74:75], v[108:109], v[66:67], v[64:65] op_sel_hi:[1,0,0]\n"
"v_pk_fma_f32 v[76:77], v[110:111], v[66:67], v[64:65] op_sel_hi:[1,0,0]\n"
"v_pk_fma_f32 v[78:79], v[112:113], v[66:67], v[64:65] op_sel_hi:[1,0,0]\n"
"v_cvt_pk_bf16_f32 v134, v72, v73\n"
"v_cvt_pk_bf16_f32 v135, v74, v75\n"
"v_cvt_pk_bf16_f32 v136, v76, v77\n"
"v_cvt_pk_bf16_f32 v137, v78, v79\n"
"v_pk_fma_f32 v[72:73], v[114:115], v[66:67], v[64:65] op_sel_hi:[1,0,0]\n"
"v_pk_fma_f32 v[74:75], v[116:117], v[66:67], v[64:65] op_sel_hi:[1,0,0]\n"
"v_pk_fma_f32 v[76:77], v[118:119], v[66:67], v[64:65] op_sel_hi:[1,0,0]\n"
"v_pk_fma_f32 v[78:79], v[120:121], v[66:67], v[64:65] op_sel_hi:[1,0,0]\n"
"v_cvt_pk_bf16_f32 v138, v72, v73\n"
"v_cvt_pk_bf16_f32 v139, v74, v75\n"
"v_cvt_pk_bf16_f32 v140, v76, v77\n"
"v_cvt_pk_bf16_f32 v141, v78, v79\n"
"v_pk_fma_f32 v[72:73], v[122:123], v[66:67], v[64:65] op_sel_hi:[1,0,0]\n"
"v_pk_fma_f32 v[74:75], v[124:125], v[66:67], v[64:65] op_sel_hi:[1,0,0]\n"
"v_cvt_pk_bf16_f32 v142, v72, v73\n"
"v_cvt_pk_bf16_f32 v143, v74, v75\n"
"v_and_or_b32 v69, v143, s82, 1.0\n"
"v_cndmask_b32 v143, v143, v69, vcc\n"
"s_and_b32 s50, s77, 1\n"
"s_lshl_b32 s50, s50, 9\n"
"s_sub_u32 s50, s50, s45\n"
"v_add_u32 v67, s50, v2\n"
"v_add_u32 v69, s50, v3\n"
"v_add_u32 v65, 0x1000, v67\n"
"global_store_dwordx4 v67, v[126:129], s[72:73] offset:0 sc1\n"
"global_store_dwordx4 v67, v[130:133], s[72:73] offset:2048 sc1\n"
"global_store_dwordx4 v65, v[134:137], s[72:73] offset:0 sc1\n"
"global_store_dwordx4 v65, v[138:141], s[72:73] offset:2048 sc1\n"
"global_store_dwordx2 v69, v[142:143], s[72:73] sc1\n"
"v_pk_fma_f32 v[72:73], v[90:91], v[66:67], v[68:69] op_sel_hi:[1,0,0]\n"
"v_pk_fma_f32 v[74:75], v[92:93], v[66:67], v[68:69] op_sel_hi:[1,0,0]\n"
"v_pk_fma_f32 v[76:77], v[94:95], v[66:67], v[68:69] op_sel_hi:[1,0,0]\n"
"v_pk_fma_f32 v[78:79], v[96:97], v[66:67], v[68:69] op_sel_hi:[1,0,0]\n"
"v_cvt_pk_bf16_f32 v24, v72, v73\n"
"v_cvt_pk_bf16_f32 v25, v74, v75\n"
"v_cvt_pk_bf16_f32 v26, v76, v77\n"
"v_cvt_pk_bf16_f32 v27, v78, v79\n"
"v_pk_fma_f32 v[72:73], v[98:99], v[66:67], v[68:69] op_sel_hi:[1,0,0]\n"
"v_pk_fma_f32 v[74:75], v[100:101], v[66:67], v[68:69] op_sel_hi:[1,0,0]\n"
"v_pk_fma_f32 v[76:77], v[102:103], v[66:67], v[68:69] op_sel_hi:[1,0,0]\n"
"v_pk_fma_f32 v[78:79], v[104:105], v[66:67], v[68:69] op_sel_hi:[1,0,0]\n"
"v_cvt_pk_bf16_f32 v28, v72, v73\n"
"v_cvt_pk_bf16_f32 v29, v74, v75\n"
"v_cvt_pk_bf16_f32 v30, v76, v77\n"
"v_cvt_pk_bf16_f32 v31, v78, v79\n"
"v_pk_fma_f32 v[72:73], v[106:107], v[66:67], v[68:69] op_sel_hi:[1,0,0]\n"
"v_pk_fma_f32 v[74:75], v[108:109], v[66:67], v[68:69] op_sel_hi:[1,0,0]\n"
"v_pk_fma_f32 v[76:77], v[110:111], v[66:67], v[68:69] op_sel_hi:[1,0,0]\n"
"v_pk_fma_f32 v[78:79], v[112:113], v[66:67], v[68:69] op_sel_hi:[1,0,0]\n"
"v_cvt_pk_bf16_f32 v32, v72, v73\n"
"v_cvt_pk_bf16_f32 v33, v74, v75\n"
"v_cvt_pk_bf16_f32 v34, v76, v77\n"
"v_cvt_pk_bf16_f32 v35, v78, v79\n"
"v_pk_fma_f32 v[72:73], v[114:115], v[66:67], v[68:69] op_sel_hi:[1,0,0]\n"
"v_pk_fma_f32 v[74:75], v[116:117], v[66:67], v[68:69] op_sel_hi:[1,0,0]\n"
"v_pk_fma_f32 v[76:77], v[118:119], v[66:67], v[68:69] op_sel_hi:[1,0,0]\n"
"v_pk_fma_f32 v[78:79], v[120:121], v[66:67], v[68:69] op_sel_hi:[1,0,0]\n"
"v_cvt_pk_bf16_f32 v36, v72, v73\n"
"v_cvt_pk_bf16_f32 v37, v74, v75\n"
"v_cvt_pk_bf16_f32 v38, v76, v77\n"
"v_cvt_pk_bf16_f32 v39, v78, v79\n"
"v_pk_fma_f32 v[72:73], v[122:123], v[66:67], v[68:69] op_sel_hi:[1,0,0]\n"
"v_pk_fma_f32 v[74:75], v[124:125], v[66:67], v[68:69] op_sel_hi:[1,0,0]\n"
"v_cvt_pk_bf16_f32 v40, v72, v73\n"
"v_cvt_pk_bf16_f32 v41, v74, v75\n"
"s_and_b32 s50, s77, 1\n"
"s_mul_i32 s50, s50, 4608\n"
"s_add_u32 s50, s50, 122368\n"
"v_add_u32 v67, s50, v10\n"
"v_add_u32 v69, s50, v23\n"
"ds_write_b128 v67, v[24:27] offset:0\n"
"ds_write_b128 v67, v[28:31] offset:1024\n"
"ds_write_b128 v67, v[32:35] offset:2048\n"
"ds_write_b128 v67, v[36:39] offset:3072\n"
"ds_write_b64 v69, v[40:41] offset:4096\n"
"s_waitcnt vmcnt(5)\n"
"v_readfirstlane_b32 s49, v239\n"
"s_cmp_eq_u32 s77, 2\n"
"s_cbranch_scc0 L_z3_%=\n"
"s_mov_b32 s50, 0xffff0000\n"
"s_waitcnt vmcnt(10)\n"
"v_lshlrev_b32 v48, 16, v44\n"
"v_and_b32 v49, s50, v44\n"
"v_lshlrev_b32 v50, 16, v45\n"
"v_and_b32 v51, s50, v45\n"
"v_lshlrev_b32 v52, 16, v46\n"
"v_and_b32 v53, s50, v46\n"
"v_lshlrev_b32 v54, 16, v47\n"
"v_and_b32 v55, s50, v47\n"
"v_pk_add_f32 v[56:57], v[48:49], v[50:51]\n"
"v_pk_mul_f32 v[58:59], v[48:49], v[48:49]\n"
"v_pk_add_f32 v[56:57], v[56:57], v[52:53]\n"
"v_pk_fma_f32 v[58:59], v[50:51], v[50:51], v[58:59]\n"
"v_pk_add_f32 v[56:57], v[56:57], v[54:55]\n"
"v_pk_fma_f32 v[58:59], v[52:53], v[52:53], v[58:59]\n"
"v_pk_fma_f32 v[58:59], v[54:55], v[54:55], v[58:59]\n"
"v_add_f32 v56, v56, v57\n"
"v_add_f32 v58, v58, v59\n"
"s_nop 0\n"
"v_add_f32_dpp v56, v56, v56 quad_perm:[1,0,3,2] row_mask:0xf bank_mask:0xf\n"
"v_add_f32_dpp v58, v58, v58 quad_perm:[1,0,3,2] row_mask:0xf bank_mask:0xf\n"
"s_nop 0\n"
"v_add_f32_dpp v56, v56, v56 quad_perm:[2,3,0,1] row_mask:0xf bank_mask:0xf\n"
"v_add_f32_dpp v58, v58, v58 quad_perm:[2,3,0,1] row_mask:0xf bank_mask:0xf\n"
"s_nop 0\n"
"v_add_f32_dpp v56, v56, v56 row_half_mirror row_mask:0xf bank_mask:0xf\n"
"v_add_f32_dpp v58, v58, v58 row_half_mirror row_mask:0xf bank_mask:0xf\n"
"s_nop 0\n"
"v_add_f32_dpp v56, v56, v56 row_mirror row_mask:0xf bank_mask:0xf\n"
"v_add_f32_dpp v58, v58, v58 row_mirror row_mask:0xf bank_mask:0xf\n"
"v_mov_b32 v57, v56\n"
"v_mov_b32 v59, v58\n"
"s_nop 0\n"
"v_permlane16_swap_b32 v56, v57\n"
"v_permlane16_swap_b32 v58, v59\n"
"v_add_f32 v56, v56, v57\n"
"v_add_f32 v58, v58, v59\n"
"v_mul_f32 v56, 0x3be38e39, v56\n"
"v_mul_f32 v58, 0x3be38e39, v58\n"
"v_fma_f32 v58, -v56, v56, v58\n"
"v_max_f32 v58, 0, v58\n"
"v_add_f32 v58, 0x3727c5ac, v58\n"
"v_rsq_f32 v58, v58\n"
"s_nop 0\n"
"v_mul_f32 v56, -v56, v58\n"
"v_pk_fma_f32 v[48:49], v[48:49], v[58:59], v[56:57] op_sel_hi:[1,0,0]\n"
"v_pk_fma_f32 v[50:51], v[50:51], v[58:59], v[56:57] op_sel_hi:[1,0,0]\n"
"v_pk_fma_f32 v[52:53], v[52:53], v[58:59], v[56:57] op_sel_hi:[1,0,0]\n"
"v_pk_fma_f32 v[54:55], v[54:55], v[58:59], v[56:57] op_sel_hi:[1,0,0]\n"
"v_cvt_pk_bf16_f32 v44, v48, v49\n"
"v_cvt_pk_bf16_f32 v45, v50, v51\n"
"v_cvt_pk_bf16_f32 v46, v52, v53\n"
"v_cvt_pk_bf16_f32 v47, v54, v55\n"
"v_and_b32 v48, 31, v0\n"
"v_lshrrev_b32 v49, 5, v0\n"
"v_mul_u32_u24 v49, 15, v49\n"
"v_sub_u32 v49, 15, v49\n"
"v_and_b32 v50, 1, v0\n"
"v_lshl_add_u32 v49, v50, 5, v49\n"
"v_lshrrev_b32 v50, 2, v48\n"
"v_lshlrev_b32 v50, 10, v50\n"
"v_bfe_u32 v51, v0, 1, 1\n"
"v_lshl_add_u32 v50, v51, 3, v50\n"
"v_lshl_add_u32 v50, v49, 4, v50\n"
"v_add_u32 v51, 0x100, v50\n"
"v_lshlrev_b32 v52, 3, v49\n"
"v_add_u32 v52, 0x1000, v52\n"
"v_add_u32 v53, 0x80, v52\n"
"v_cmp_gt_u32 vcc, 16, v48\n"
"s_nop 1\n"
"v_cndmask_b32 v50, v52, v50, vcc\n"
"v_cndmask_b32 v51, v53, v51, vcc\n"
"v_add_u32 v50, 131584, v50\n"
"v_add_u32 v51, 131584, v51\n"
"s_mov_b32 exec_lo, 0x3ffff\n"
"s_mov_b32 exec_hi, 0x3ffff\n"
"ds_write_b64 v50, v[44:45]\n"
"ds_write_b64 v51, v[46:47]\n"
"s_mov_b64 exec, -1\n"
"s_branch L_zj_%=\n"
"L_z3_%=:\n"
"s_mov_b32 m0, 154624\n"
"s_mov_b32 s50, 0\n"
"buffer_load_dwordx4 v235, s[28:31], s50 offen lds\n"
"s_mov_b32 m0, 155648\n"
"s_mov_b32 s50, 9216\n"
"buffer_load_dwordx4 v235, s[28:31], s50 offen lds\n"
"s_mov_b32 m0, 156672\n"
"s_mov_b32 s50, 18432\n"
"buffer_load_dwordx4 v235, s[28:31], s50 offen lds\n"
"s_mov_b32 m0, 157696\n"
"s_mov_b32 s50, 27648\n"
"buffer_load_dwordx4 v235, s[28:31], s50 offen lds\n"
"s_mov_b32 m0, 158720\n"
"s_mov_b32 s50, 36864\n"
"buffer_load_dwordx4 v235, s[28:31], s50 offen lds\n"
"s_mov_b32 m0, 159744\n"
"s_mov_b32 s50, 46080\n"
"buffer_load_dwordx4 v235, s[28:31], s50 offen lds\n"
"s_mov_b32 m0, 160768\n"
"s_mov_b32 s50, 55296\n"
"buffer_load_dwordx4 v235, s[28:31], s50 offen lds\n"
"s_mov_b32 m0, 161792\n"
"s_mov_b32 s50, 64512\n"
"buffer_load_dwordx4 v235, s[28:31], s50 offen lds\n"
"s_mov_b32 m0, 162816\n"
"s_mov_b32 s50, 73728\n"
"buffer_load_dwordx4 v235, s[28:31], s50 offen lds\n"
"L_zj_%=:\n"
"s_barrier\n"
"s_add_u32 s49, s49, 1\n"
"v_mov_b32 v64, s35\n"
"v_mov_b32 v65, s49\n"
"s_cmp_ge_u32 s77, 2\n"
"ds_read_b64_tr_b16 a[156:157], v70 offset:0\n"
"ds_read_b64_tr_b16 a[158:159], v70 offset:4608\n"
"ds_read_b64_tr_b16 a[160:161], v70 offset:9216\n"
"ds_read_b64_tr_b16 a[162:163], v70 offset:13824\n"
"ds_read_b64_tr_b16 a[164:165], v70 offset:18432\n"
"ds_read_b64_tr_b16 a[166:167], v70 offset:23040\n"
"ds_read_b64_tr_b16 a[168:169], v70 offset:27648\n"
"ds_read_b64_tr_b16 a[170:171], v70 offset:32256\n"
"ds_read_b64_tr_b16 a[188:189], v70 offset:36864\n"
"ds_read_b64_tr_b16 a[172:173], v70 offset:32\n"
"ds_read_b64_tr_b16 a[174:175], v70 offset:4640\n"
"s_waitcnt lgkmcnt(9)\n"
"v_mfma_f32_16x16x32_bf16 v[90:93], a[156:159], v[24:27], 0\n"
"s_waitcnt lgkmcnt(7)\n"
"ds_read_b64_tr_b16 a[176:177], v70 offset:9248\n"
"ds_read_b64_tr_b16 a[178:179], v70 offset:13856\n"
"v_mfma_f32_16x16x32_bf16 v[90:93], a[160:163], v[28:31], v[90:93]\n"
"s_waitcnt lgkmcnt(7)\n"
"ds_read_b64_tr_b16 a[180:181], v70 offset:18464\n"
"ds_read_b64_tr_b16 a[182:183], v70 offset:23072\n"
"v_mfma_f32_16x16x32_bf16 v[90:93], a[164:167], v[32:35], v[90:93]\n"
"s_waitcnt lgkmcnt(7)\n"
"ds_read_b64_tr_b16 a[184:185], v70 offset:27680\n"
"ds_read_b64_tr_b16 a[186:187], v70 offset:32288\n"
"v_mfma_f32_16x16x32_bf16 v[90:93], a[168:171], v[36:39], v[90:93]\n"
"s_waitcnt lgkmcnt(8)\n"
"ds_read_b64_tr_b16 a[192:193], v70 offset:36896\n"
"v_mfma_f32_16x16x32_bf16 v[90:93], a[188:191], v[40:43], v[90:93]\n"
"s_waitcnt lgkmcnt(7)\n"
"ds_read_b64_tr_b16 a[156:157], v70 offset:64\n"
"ds_read_b64_tr_b16 a[158:159], v70 offset:4672\n"
"v_mfma_f32_16x16x32_bf16 v[94:97], a[172:175], v[24:27], 0\n"
"s_waitcnt lgkmcnt(7)\n"
"ds_read_b64_tr_b16 a[160:161], v70 offset:9280\n"
"ds_read_b64_tr_b16 a[162:163], v70 offset:13888\n"
"v_mfma_f32_16x16x32_bf16 v[94:97], a[176:179], v[28:31], v[94:97]\n"
"s_waitcnt lgkmcnt(7)\n"
"ds_read_b64_tr_b16 a[164:165], v70 offset:18496\n"
"ds_read_b64_tr_b16 a[166:167], v70 offset:23104\n"
"v_mfma_f32_16x16x32_bf16 v[94:97], a[180:183], v[32:35], v[94:97]\n"
"s_waitcnt lgkmcnt(7)\n"
"ds_read_b64_tr_b16 a[168:169], v70 offset:27712\n"
"ds_read_b64_tr_b16 a[170:171], v70 offset:32320\n"
"v_mfma_f32_16x16x32_bf16 v[94:97], a[184:187], v[36:39], v[94:97]\n"
"s_waitcnt lgkmcnt(8)\n"
"ds_read_b64_tr_b16 a[188:189], v70 offset:36928\n"
"v_mfma_f32_16x16x32_bf16 v[94:97], a[192:195], v[40:43], v[94:97]\n"
"s_waitcnt lgkmcnt(7)\n"
"ds_read_b64_tr_b16 a[172:173], v70 offset:96\n"
"ds_read_b64_tr_b16 a[174:175], v70 offset:4704\n"
"v_mfma_f32_16x16x32_bf16 v[98:101], a[156:159], v[24:27], 0\n"
"s_waitcnt lgkmcnt(7)\n"
"ds_read_b64_tr_b16 a[176:177], v70 offset:9312\n"
"ds_read_b64_tr_b16 a[178:179], v70 offset:13920\n"
"v_mfma_f32_16x16x32_bf16 v[98:101], a[160:163], v[28:31], v[98:101]\n"
"s_waitcnt lgkmcnt(7)\n"
"ds_read_b64_tr_b16 a[180:181], v70 offset:18528\n"
"ds_read_b64_tr_b16 a[182:183], v70 offset:23136\n"
"v_mfma_f32_16x16x32_bf16 v[98:101], a[164:167], v[32:35], v[98:101]\n"
"s_waitcnt lgkmcnt(7)\n"
"ds_read_b64_tr_b16 a[184:185], v70 offset:27744\n"
"ds_read_b64_tr_b16 a[186:187], v70 offset:32352\n"
"v_mfma_f32_16x16x32_bf16 v[98:101], a[168:171], v[36:39], v[98:101]\n"
"s_waitcnt lgkmcnt(8)\n"
"ds_read_b64_tr_b16 a[192:193], v70 offset:36960\n"
"v_mfma_f32_16x16x32_bf16 v[98:101], a[188:191], v[40:43], v[98:101]\n"
"s_waitcnt lgkmcnt(7)\n"
"ds_read_b64_tr_b16 a[156:157], v70 offset:128\n"
"ds_read_b64_tr_b16 a[158:159], v70 offset:4736\n"
"v_mfma_f32_16x16x32_bf16 v[102:105], a[172:175], v[24:27], 0\n"
"s_cbranch_scc0 L_fl_%=\n"
"s_waitcnt vmcnt(9)\n"
"s_cmp_eq_u32 s77, 3\n"
"s_cbranch_scc1 L_fm_%=\n"
"s_waitcnt vmcnt(0)\n"
"L_fm_%=:\n"
"s_mov_b64 exec, 1\n"
"global_store_dword v64, v65, s[66:67] sc1\n"
"s_mov_b64 exec, -1\n"
"L_fl_%=:\n"
"s_waitcnt lgkmcnt(7)\n"
"ds_read_b64_tr_b16 a[160:161], v70 offset:9344\n"
"ds_read_b64_tr_b16 a[162:163], v70 offset:13952\n"
"v_mfma_f32_16x16x32_bf16 v[102:105], a[176:179], v[28:31], v[102:105]\n"
"s_waitcnt lgkmcnt(7)\n"
"ds_read_b64_tr_b16 a[164:165], v70 offset:18560\n"
"ds_read_b64_tr_b16 a[166:167], v70 offset:23168\n"
"v_mfma_f32_16x16x32_bf16 v[102:105], a[180:183], v[32:35], v[102:105]\n"
"s_waitcnt lgkmcnt(7)\n"
"ds_read_b64_tr_b16 a[168:169], v70 offset:27776\n"
"ds_read_b64_tr_b16 a[170:171], v70 offset:32384\n"
"v_mfma_f32_16x16x32_bf16 v[102:105], a[184:187], v[36:39], v[102:105]\n"
"s_waitcnt lgkmcnt(8)\n"
"ds_read_b64_tr_b16 a[188:189], v70 offset:36992\n"
"v_mfma_f32_16x16x32_bf16 v[102:105], a[192:195], v[40:43], v[102:105]\n"
"s_waitcnt lgkmcnt(7)\n"
"ds_read_b64_tr_b16 a[172:173], v70 offset:160\n"
"ds_read_b64_tr_b16 a[174:175], v70 offset:4768\n"
"v_mfma_f32_16x16x32_bf16 v[106:109], a[156:159], v[24:27], 0\n"
"s_waitcnt lgkmcnt(7)\n"
"ds_read_b64_tr_b16 a[176:177], v70 offset:9376\n"
"ds_read_b64_tr_b16 a[178:179], v70 offset:13984\n"
"v_mfma_f32_16x16x32_bf16 v[106:109], a[160:163], v[28:31], v[106:109]\n"
"s_waitcnt lgkmcnt(7)\n"
"ds_read_b64_tr_b16 a[180:181], v70 offset:18592\n"
"ds_read_b64_tr_b16 a[182:183], v70 offset:23200\n"
"v_mfma_f32_16x16x32_bf16 v[106:109], a[164:167], v[32:35], v[106:109]\n"
"s_waitcnt lgkmcnt(7)\n"
"ds_read_b64_tr_b16 a[184:185], v70 offset:27808\n"
"ds_read_b64_tr_b16 a[186:187], v70 offset:32416\n"
"v_mfma_f32_16x16x32_bf16 v[106:109], a[168:171], v[36:39], v[106:109]\n"
"s_waitcnt lgkmcnt(8)\n"
"ds_read_b64_tr_b16 a[192:193], v70 offset:37024\n"
"v_mfma_f32_16x16x32_bf16 v[106:109], a[188:191], v[40:43], v[106:109]\n"
"s_waitcnt lgkmcnt(7)\n"
"ds_read_b64_tr_b16 a[156:157], v70 offset:192\n"
"ds_read_b64_tr_b16 a[158:159], v70 offset:4800\n"
"v_mfma_f32_16x16x32_bf16 v[110:113], a[172:175], v[24:27], 0\n"
"s_waitcnt lgkmcnt(7)\n"
"ds_read_b64_tr_b16 a[160:161], v70 offset:9408\n"
"ds_read_b64_tr_b16 a[162:163], v70 offset:14016\n"
"v_mfma_f32_16x16x32_bf16 v[110:113], a[176:179], v[28:31], v[110:113]\n"
"s_waitcnt lgkmcnt(7)\n"
"ds_read_b64_tr_b16 a[164:165], v70 offset:18624\n"
"ds_read_b64_tr_b16 a[166:167], v70 offset:23232\n"
"v_mfma_f32_16x16x32_bf16 v[110:113], a[180:183], v[32:35], v[110:113]\n"
"s_waitcnt lgkmcnt(7)\n"
"ds_read_b64_tr_b16 a[168:169], v70 offset:27840\n"
"ds_read_b64_tr_b16 a[170:171], v70 offset:32448\n"
"v_mfma_f32_16x16x32_bf16 v[110:113], a[184:187], v[36:39], v[110:113]\n"
"s_waitcnt lgkmcnt(8)\n"
"ds_read_b64_tr_b16 a[188:189], v70 offset:37056\n"
"v_mfma_f32_16x16x32_bf16 v[110:113], a[192:195], v[40:43], v[110:113]\n"
"s_waitcnt lgkmcnt(7)\n"
"ds_read_b64_tr_b16 a[172:173], v70 offset:224\n"
"ds_read_b64_tr_b16 a[174:175], v70 offset:4832\n"
"v_mfma_f32_16x16x32_bf16 v[114:117], a[156:159], v[24:27], 0\n"
"s_waitcnt lgkmcnt(7)\n"
"ds_read_b64_tr_b16 a[176:177], v70 offset:9440\n"
"ds_read_b64_tr_b16 a[178:179], v70 offset:14048\n"
"v_mfma_f32_16x16x32_bf16 v[114:117], a[160:163], v[28:31], v[114:117]\n"
"s_waitcnt lgkmcnt(7)\n"
"ds_read_b64_tr_b16 a[180:181], v70 offset:18656\n"
"ds_read_b64_tr_b16 a[182:183], v70 offset:23264\n"
"v_mfma_f32_16x16x32_bf16 v[114:117], a[164:167], v[32:35], v[114:117]\n"
"s_waitcnt lgkmcnt(7)\n"
"ds_read_b64_tr_b16 a[184:185], v70 offset:27872\n"
"ds_read_b64_tr_b16 a[186:187], v70 offset:32480\n"
"v_mfma_f32_16x16x32_bf16 v[114:117], a[168:171], v[36:39], v[114:117]\n"
"s_waitcnt lgkmcnt(8)\n"
"ds_read_b64_tr_b16 a[192:193], v70 offset:37088\n"
"v_mfma_f32_16x16x32_bf16 v[114:117], a[188:191], v[40:43], v[114:117]\n"
"s_waitcnt lgkmcnt(7)\n"
"ds_read_b64_tr_b16 a[156:157], v70 offset:256\n"
"ds_read_b64_tr_b16 a[158:159], v70 offset:4864\n"
"v_mfma_f32_16x16x32_bf16 v[118:121], a[172:175], v[24:27], 0\n"
"s_waitcnt lgkmcnt(7)\n"
"ds_read_b64_tr_b16 a[160:161], v70 offset:9472\n"
"ds_read_b64_tr_b16 a[162:163], v70 offset:14080\n"
"v_mfma_f32_16x16x32_bf16 v[118:121], a[176:179], v[28:31], v[118:121]\n"
"s_waitcnt lgkmcnt(7)\n"
"ds_read_b64_tr_b16 a[164:165], v70 offset:18688\n"
"ds_read_b64_tr_b16 a[166:167], v70 offset:23296\n"
"v_mfma_f32_16x16x32_bf16 v[118:121], a[180:183], v[32:35], v[118:121]\n"
"s_waitcnt lgkmcnt(7)\n"
"ds_read_b64_tr_b16 a[168:169], v70 offset:27904\n"
"ds_read_b64_tr_b16 a[170:171], v70 offset:32512\n"
"v_mfma_f32_16x16x32_bf16 v[118:121], a[184:187], v[36:39], v[118:121]\n"
"s_waitcnt lgkmcnt(8)\n"
"ds_read_b64_tr_b16 a[188:189], v70 offset:37120\n"
"v_mfma_f32_16x16x32_bf16 v[118:121], a[192:195], v[40:43], v[118:121]\n"
"s_waitcnt lgkmcnt(7)\n"
"v_mfma_f32_16x16x32_bf16 v[122:125], a[156:159], v[24:27], 0\n"
"s_waitcnt lgkmcnt(5)\n"
"v_mfma_f32_16x16x32_bf16 v[122:125], a[160:163], v[28:31], v[122:125]\n"
"s_waitcnt lgkmcnt(3)\n"
"v_mfma_f32_16x16x32_bf16 v[122:125], a[164:167], v[32:35], v[122:125]\n"
"s_waitcnt lgkmcnt(1)\n"
"v_mfma_f32_16x16x32_bf16 v[122:125], a[168:171], v[36:39], v[122:125]\n"
"s_waitcnt lgkmcnt(0)\n"
"v_mfma_f32_16x16x32_bf16 v[122:125], a[188:191], v[40:43], v[122:125]\n"
"s_waitcnt vmcnt(10)\n"
"v_cvt_pk_bf16_f32 v44, v90, v91\n"
"v_cvt_pk_bf16_f32 v45, v92, v93\n"
"v_cvt_pk_bf16_f32 v46, v94, v95\n"
"v_cvt_pk_bf16_f32 v47, v96, v97\n"
"v_cvt_pk_bf16_f32 v48, v98, v99\n"
"v_cvt_pk_bf16_f32 v49, v100, v101\n"
"v_cvt_pk_bf16_f32 v50, v102, v103\n"
"v_cvt_pk_bf16_f32 v51, v104, v105\n"
"v_cvt_pk_bf16_f32 v52, v106, v107\n"
"v_cvt_pk_bf16_f32 v53, v108, v109\n"
"v_cvt_pk_bf16_f32 v54, v110, v111\n"
"v_cvt_pk_bf16_f32 v55, v112, v113\n"
"v_cvt_pk_bf16_f32 v56, v114, v115\n"
"v_cvt_pk_bf16_f32 v57, v116, v117\n"
"v_cvt_pk_bf16_f32 v58, v118, v119\n"
"v_cvt_pk_bf16_f32 v59, v120, v121\n"
"v_cvt_pk_bf16_f32 v60, v122, v123\n"
"v_cvt_pk_bf16_f32 v61, v124, v125\n"
"s_cmp_ge_u32 s77, 2\n"
"s_cbranch_scc1 L_k2_%=\n"
"s_waitcnt vmcnt(0)\n"
"L_k2_%=:\n"
"s_barrier\n"
"s_lshl_b32 s21, s77, 6\n"
"s_sub_u32 s20, s79, 14\n"
"s_mul_i32 s20, s20, 36864\n"
"s_add_u32 s20, s20, s45\n"
"s_sub_u32 s23, s20, 36864\n"
"ds_read_b64 a[156:157], v71 offset:0\n"
"ds_read_b64 a[158:159], v71 offset:32\n"
"ds_read_b64 a[160:161], v71 offset:64\n"
"ds_read_b64 a[162:163], v71 offset:96\n"
"ds_read_b64 a[164:165], v71 offset:128\n"
"ds_read_b64 a[166:167], v71 offset:160\n"
"ds_read_b64 a[168:169], v71 offset:192\n"
"ds_read_b64 a[170:171], v71 offset:224\n"
"ds_read_b64 a[188:189], v71 offset:256\n"
"ds_read_b64 a[172:173], v71 offset:4864\n"
"ds_read_b64 a[174:175], v71 offset:4896\n"
"s_waitcnt lgkmcnt(9)\n"
"v_mfma_f32_16x16x32_bf16 v[90:93], a[156:159], v[44:47], 0\n"
"s_waitcnt lgkmcnt(7)\n"
"ds_read_b64 a[176:177], v71 offset:4928\n"
"ds_read_b64 a[178:179], v71 offset:4960\n"
"v_mfma_f32_16x16x32_bf16 v[90:93], a[160:163], v[48:51], v[90:93]\n"
"s_waitcnt lgkmcnt(7)\n"
"ds_read_b64 a[180:181], v71 offset:4992\n"
"ds_read_b64 a[182:183], v71 offset:5024\n"
"v_mfma_f32_16x16x32_bf16 v[90:93], a[164:167], v[52:55], v[90:93]\n"
"s_waitcnt lgkmcnt(7)\n"
"ds_read_b64 a[184:185], v71 offset:5056\n"
"ds_read_b64 a[186:187], v71 offset:5088\n"
"v_mfma_f32_16x16x32_bf16 v[90:93], a[168:171], v[56:59], v[90:93]\n"
"s_add_u32 m0, s23, 0\n"
"s_add_u32 s51, s21, 288\n"
"s_waitcnt lgkmcnt(8)\n"
"ds_read_b64 a[192:193], v71 offset:5120\n"
"v_mfma_f32_16x16x32_bf16 v[90:93], a[188:191], v[60:63], v[90:93]\n"
"buffer_load_dwordx4 v252, s[28:31], s51 offen offset:0 lds\n"
"s_waitcnt lgkmcnt(7)\n"
"ds_read_b64 a[156:157], v71 offset:9728\n"
"ds_read_b64 a[158:159], v71 offset:9760\n"
"v_mfma_f32_16x16x32_bf16 v[94:97], a[172:175], v[44:47], 0\n"
"s_waitcnt lgkmcnt(7)\n"
"ds_read_b64 a[160:161], v71 offset:9792\n"
"ds_read_b64 a[162:163], v71 offset:9824\n"
"v_mfma_f32_16x16x32_bf16 v[94:97], a[176:179], v[48:51], v[94:97]\n"
"s_waitcnt lgkmcnt(7)\n"
"ds_read_b64 a[164:165], v71 offset:9856\n"
"ds_read_b64 a[166:167], v71 offset:9888\n"
"v_mfma_f32_16x16x32_bf16 v[94:97], a[180:183], v[52:55], v[94:97]\n"
"s_waitcnt lgkmcnt(7)\n"
"ds_read_b64 a[168:169], v71 offset:9920\n"
"ds_read_b64 a[170:171], v71 offset:9952\n"
"v_mfma_f32_16x16x32_bf16 v[94:97], a[184:187], v[56:59], v[94:97]\n"
"s_add_u32 m0, s23, 0\n"
"s_add_u32 s51, s21, 8480\n"
"s_waitcnt lgkmcnt(8)\n"
"ds_read_b64 a[188:189], v71 offset:9984\n"
"v_mfma_f32_16x16x32_bf16 v[94:97], a[192:195], v[60:63], v[94:97]\n"
"buffer_load_dwordx4 v252, s[28:31], s51 offen offset:1024 lds\n"
"s_waitcnt lgkmcnt(7)\n"
"ds_read_b64 a[172:173], v71 offset:14592\n"
"ds_read_b64 a[174:175], v71 offset:14624\n"
"v_mfma_f32_16x16x32_bf16 v[98:101], a[156:159], v[44:47], 0\n"
"s_waitcnt lgkmcnt(7)\n"
"ds_read_b64 a[176:177], v71 offset:14656\n"
"ds_read_b64 a[178:179], v71 offset:14688\n"
"v_mfma_f32_16x16x32_bf16 v[98:101], a[160:163], v[48:51], v[98:101]\n"
"s_waitcnt lgkmcnt(7)\n"
"ds_read_b64 a[180:181], v71 offset:14720\n"
"ds_read_b64 a[182:183], v71 offset:14752\n"
"v_mfma_f32_16x16x32_bf16 v[98:101], a[164:167], v[52:55], v[98:101]\n"
"s_waitcnt lgkmcnt(7)\n"
"ds_read_b64 a[184:185], v71 offset:14784\n"
"ds_read_b64 a[186:187], v71 offset:14816\n"
"v_mfma_f32_16x16x32_bf16 v[98:101], a[168:171], v[56:59], v[98:101]\n"
"s_add_u32 m0, s23, 0\n"
"s_add_u32 s51, s21, 16672\n"
"s_waitcnt lgkmcnt(8)\n"
"ds_read_b64 a[192:193], v71 offset:14848\n"
"v_mfma_f32_16x16x32_bf16 v[98:101], a[188:191], v[60:63], v[98:101]\n"
"buffer_load_dwordx4 v252, s[28:31], s51 offen offset:2048 lds\n"
"s_waitcnt lgkmcnt(7)\n"
"ds_read_b64 a[156:157], v71 offset:19456\n"
"ds_read_b64 a[158:159], v71 offset:19488\n"
"v_mfma_f32_16x16x32_bf16 v[102:105], a[172:175], v[44:47], 0\n"
"s_waitcnt lgkmcnt(7)\n"
"ds_read_b64 a[160:161], v71 offset:19520\n"
"ds_read_b64 a[162:163], v71 offset:19552\n"
"v_mfma_f32_16x16x32_bf16 v[102:105], a[176:179], v[48:51], v[102:105]\n"
"s_waitcnt lgkmcnt(7)\n"
"ds_read_b64 a[164:165], v71 offset:19584\n"
"ds_read_b64 a[166:167], v71 offset:19616\n"
"v_mfma_f32_16x16x32_bf16 v[102:105], a[180:183], v[52:55], v[102:105]\n"
"s_waitcnt lgkmcnt(7)\n"
"ds_read_b64 a[168:169], v71 offset:19648\n"
"ds_read_b64 a[170:171], v71 offset:19680\n"
"v_mfma_f32_16x16x32_bf16 v[102:105], a[184:187], v[56:59], v[102:105]\n"
"s_add_u32 m0, s23, 0\n"
"s_add_u32 s51, s21, 24864\n"
"s_waitcnt lgkmcnt(8)\n"
"ds_read_b64 a[188:189], v71 offset:19712\n"
"v_mfma_f32_16x16x32_bf16 v[102:105], a[192:195], v[60:63], v[102:105]\n"
"buffer_load_dwordx4 v252, s[28:31], s51 offen offset:3072 lds\n"
"s_waitcnt lgkmcnt(7)\n"
"ds_read_b64 a[172:173], v71 offset:24320\n"
"ds_read_b64 a[174:175], v71 offset:24352\n"
"v_mfma_f32_16x16x32_bf16 v[106:109], a[156:159], v[44:47], 0\n"
"s_waitcnt lgkmcnt(7)\n"
"ds_read_b64 a[176:177], v71 offset:24384\n"
"ds_read_b64 a[178:179], v71 offset:24416\n"
"v_mfma_f32_16x16x32_bf16 v[106:109], a[160:163], v[48:51], v[106:109]\n"
"s_waitcnt lgkmcnt(7)\n"
"ds_read_b64 a[180:181], v71 offset:24448\n"
"ds_read_b64 a[182:183], v71 offset:24480\n"
"v_mfma_f32_16x16x32_bf16 v[106:109], a[164:167], v[52:55], v[106:109]\n"
"s_waitcnt lgkmcnt(7)\n"
"ds_read_b64 a[184:185], v71 offset:24512\n"
"ds_read_b64 a[186:187], v71 offset:24544\n"
"v_mfma_f32_16x16x32_bf16 v[106:109], a[168:171], v[56:59], v[106:109]\n"
"s_add_u32 m0, s23, 4096\n"
"s_add_u32 s51, s21, 37152\n"
"s_waitcnt lgkmcnt(8)\n"
"ds_read_b64 a[192:193], v71 offset:24576\n"
"v_mfma_f32_16x16x32_bf16 v[106:109], a[188:191], v[60:63], v[106:109]\n"
"buffer_load_dwordx4 v252, s[28:31], s51 offen offset:0 lds\n"
"s_waitcnt lgkmcnt(7)\n"
"ds_read_b64 a[156:157], v71 offset:29184\n"
"ds_read_b64 a[158:159], v71 offset:29216\n"
"v_mfma_f32_16x16x32_bf16 v[110:113], a[172:175], v[44:47], 0\n"
"s_waitcnt lgkmcnt(7)\n"
"ds_read_b64 a[160:161], v71 offset:29248\n"
"ds_read_b64 a[162:163], v71 offset:29280\n"
"v_mfma_f32_16x16x32_bf16 v[110:113], a[176:179], v[48:51], v[110:113]\n"
"s_waitcnt lgkmcnt(7)\n"
"ds_read_b64 a[164:165], v71 offset:29312\n"
"ds_read_b64 a[166:167], v71 offset:29344\n"
"v_mfma_f32_16x16x32_bf16 v[110:113], a[180:183], v[52:55], v[110:113]\n"
"s_waitcnt lgkmcnt(7)\n"
"ds_read_b64 a[168:169], v71 offset:29376\n"
"ds_read_b64 a[170:171], v71 offset:29408\n"
"v_mfma_f32_16x16x32_bf16 v[110:113], a[184:187], v[56:59], v[110:113]\n"
"s_add_u32 m0, s23, 4096\n"
"s_add_u32 s51, s21, 45344\n"
"s_waitcnt lgkmcnt(8)\n"
"ds_read_b64 a[188:189], v71 offset:29440\n"
"v_mfma_f32_16x16x32_bf16 v[110:113], a[192:195], v[60:63], v[110:113]\n"
"buffer_load_dwordx4 v252, s[28:31], s51 offen offset:1024 lds\n"
"s_waitcnt lgkmcnt(7)\n"
"ds_read_b64 a[172:173], v71 offset:34048\n"
"ds_read_b64 a[174:175], v71 offset:34080\n"
"v_mfma_f32_16x16x32_bf16 v[114:117], a[156:159], v[44:47], 0\n"
"s_waitcnt lgkmcnt(7)\n"
"ds_read_b64 a[176:177], v71 offset:34112\n"
"ds_read_b64 a[178:179], v71 offset:34144\n"
"v_mfma_f32_16x16x32_bf16 v[114:117], a[160:163], v[48:51], v[114:117]\n"
"s_waitcnt lgkmcnt(7)\n"
"ds_read_b64 a[180:181], v71 offset:34176\n"
"ds_read_b64 a[182:183], v71 offset:34208\n"
"v_mfma_f32_16x16x32_bf16 v[114:117], a[164:167], v[52:55], v[114:117]\n"
"s_waitcnt lgkmcnt(7)\n"
"ds_read_b64 a[184:185], v71 offset:34240\n"
"ds_read_b64 a[186:187], v71 offset:34272\n"
"v_mfma_f32_16x16x32_bf16 v[114:117], a[168:171], v[56:59], v[114:117]\n"
"s_add_u32 m0, s23, 4096\n"
"s_add_u32 s51, s21, 53536\n"
"s_waitcnt lgkmcnt(8)\n"
"ds_read_b64 a[192:193], v71 offset:34304\n"
"v_mfma_f32_16x16x32_bf16 v[114:117], a[188:191], v[60:63], v[114:117]\n"
"buffer_load_dwordx4 v252, s[28:31], s51 offen offset:2048 lds\n"
"s_waitcnt lgkmcnt(7)\n"
"ds_read_b64 a[156:157], v71 offset:38912\n"
"ds_read_b64 a[158:159], v71 offset:38944\n"
"v_mfma_f32_16x16x32_bf16 v[118:121], a[172:175], v[44:47], 0\n"
"s_waitcnt lgkmcnt(7)\n"
"ds_read_b64 a[160:161], v71 offset:38976\n"
"ds_read_b64 a[162:163], v71 offset:39008\n"
"v_mfma_f32_16x16x32_bf16 v[118:121], a[176:179], v[48:51], v[118:121]\n"
"s_waitcnt lgkmcnt(7)\n"
"ds_read_b64 a[164:165], v71 offset:39040\n"
"ds_read_b64 a[166:167], v71 offset:39072\n"
"v_mfma_f32_16x16x32_bf16 v[118:121], a[180:183], v[52:55], v[118:121]\n"
"s_waitcnt lgkmcnt(7)\n"
"ds_read_b64 a[168:169], v71 offset:39104\n"
"ds_read_b64 a[170:171], v71 offset:39136\n"
"v_mfma_f32_16x16x32_bf16 v[118:121], a[184:187], v[56:59], v[118:121]\n"
"s_add_u32 m0, s23, 4096\n"
"s_add_u32 s51, s21, 61728\n"
"s_waitcnt lgkmcnt(8)\n"
"ds_read_b64 a[188:189], v71 offset:39168\n"
"v_mfma_f32_16x16x32_bf16 v[118:121], a[192:195], v[60:63], v[118:121]\n"
"buffer_load_dwordx4 v252, s[28:31], s51 offen offset:3072 lds\n"
"s_waitcnt lgkmcnt(7)\n"
"v_mfma_f32_16x16x32_bf16 v[122:125], a[156:159], v[44:47], 0\n"
"s_waitcnt lgkmcnt(5)\n"
"v_mfma_f32_16x16x32_bf16 v[122:125], a[160:163], v[48:51], v[122:125]\n"
"s_waitcnt lgkmcnt(3)\n"
"v_mfma_f32_16x16x32_bf16 v[122:125], a[164:167], v[52:55], v[122:125]\n"
"s_waitcnt lgkmcnt(1)\n"
"v_mfma_f32_16x16x32_bf16 v[122:125], a[168:171], v[56:59], v[122:125]\n"
"s_add_u32 m0, s23, 8192\n"
"s_add_u32 s51, s21, 74016\n"
"s_waitcnt lgkmcnt(0)\n"
"v_mfma_f32_16x16x32_bf16 v[122:125], a[188:191], v[60:63], v[122:125]\n"
"buffer_load_dwordx4 v252, s[28:31], s51 offen offset:0 lds\n"
"global_load_dword v20, v19, s[66:67] sc1\n"
"s_cmp_eq_u32 s77, 3\n"
"s_cbranch_scc0 L_e0w_%=\n"
"s_waitcnt vmcnt(10)\n"
"L_e0w_%=:\n"
"v_pk_add_f32 v[64:65], v[90:91], v[92:93]\n"
"v_pk_add_f32 v[60:61], v[94:95], v[96:97]\n"
"v_pk_add_f32 v[64:65], v[64:65], v[98:99]\n"
"v_pk_add_f32 v[60:61], v[60:61], v[100:101]\n"
"v_pk_add_f32 v[64:65], v[64:65], v[102:103]\n"
"v_pk_add_f32 v[60:61], v[60:61], v[104:105]\n"
"v_pk_add_f32 v[64:65], v[64:65], v[106:107]\n"
"v_pk_add_f32 v[60:61], v[60:61], v[108:109]\n"
"v_pk_add_f32 v[64:65], v[64:65], v[110:111]\n"
"v_pk_add_f32 v[60:61], v[60:61], v[112:113]\n"
"v_pk_add_f32 v[64:65], v[64:65], v[114:115]\n"
"v_pk_add_f32 v[60:61], v[60:61], v[116:117]\n"
"v_pk_add_f32 v[64:65], v[64:65], v[118:119]\n"
"v_pk_add_f32 v[60:61], v[60:61], v[120:121]\n"
"v_pk_add_f32 v[64:65], v[64:65], v[122:123]\n"
"v_pk_add_f32 v[60:61], v[60:61], v[124:125]\n"
"v_cmp_le_u32 s[56:57], 48, v0\n"
"v_pk_add_f32 v[64:65], v[64:65], v[60:61]\n"
"v_add_f32 v64, v64, v65\n"
"v_cndmask_b32 v65, 0, v125, s[56:57]\n"
"v_mov_b32 v68, 0xbdaaaaab\n"
"v_fmac_f32 v65, v68, v64\n"
"s_mul_i32 s51, s77, 4608\n"
"s_add_u32 s51, s51, 136192\n"
"v_mfma_f32_16x16x4_f32 v[24:27], v234, v65, 0\n"
"v_mov_b32 v58, 0x3df6384f\n"
"v_add_u32 v56, s51, v10\n"
"v_add_u32 v57, s51, v23\n"
"s_lshl_b32 s50, s77, 2\n"
"s_add_u32 s50, s50, 122112\n"
"v_mov_b32 v70, s50\n"
"v_mov_b32 v54, 122128\n"
"v_mov_b32 v55, 0\n"
"v_pk_mul_f32 v[66:67], v[90:91], v[90:91]\n"
"v_pk_mul_f32 v[62:63], v[92:93], v[92:93]\n"
"v_pk_fma_f32 v[66:67], v[94:95], v[94:95], v[66:67]\n"
"v_pk_fma_f32 v[62:63], v[96:97], v[96:97], v[62:63]\n"
"v_mul_f32 v64, 0x3c3311ae, v24\n"
"v_pk_fma_f32 v[90:91], v[90:91], v[58:59], v[64:65] op_sel_hi:[1,0,0]\n"
"v_pk_fma_f32 v[92:93], v[92:93], v[58:59], v[64:65] op_sel_hi:[1,0,0]\n"
"v_pk_fma_f32 v[94:95], v[94:95], v[58:59], v[64:65] op_sel_hi:[1,0,0]\n"
"v_pk_fma_f32 v[96:97], v[96:97], v[58:59], v[64:65] op_sel_hi:[1,0,0]\n"
"v_cvt_pk_bf16_f32 v72, v90, v91\n"
"v_cvt_pk_bf16_f32 v73, v92, v93\n"
"v_cvt_pk_bf16_f32 v74, v94, v95\n"
"v_cvt_pk_bf16_f32 v75, v96, v97\n"
"ds_write_b128 v56, v[72:75] offset:0\n"
"v_pk_fma_f32 v[66:67], v[98:99], v[98:99], v[66:67]\n"
"v_pk_fma_f32 v[62:63], v[100:101], v[100:101], v[62:63]\n"
"v_pk_fma_f32 v[66:67], v[102:103], v[102:103], v[66:67]\n"
"v_pk_fma_f32 v[62:63], v[104:105], v[104:105], v[62:63]\n"
"v_pk_fma_f32 v[98:99], v[98:99], v[58:59], v[64:65] op_sel_hi:[1,0,0]\n"
"v_pk_fma_f32 v[100:101], v[100:101], v[58:59], v[64:65] op_sel_hi:[1,0,0]\n"
"v_pk_fma_f32 v[102:103], v[102:103], v[58:59], v[64:65] op_sel_hi:[1,0,0]\n"
"v_pk_fma_f32 v[104:105], v[104:105], v[58:59], v[64:65] op_sel_hi:[1,0,0]\n"
"v_cvt_pk_bf16_f32 v76, v98, v99\n"
"v_cvt_pk_bf16_f32 v77, v100, v101\n"
"v_cvt_pk_bf16_f32 v78, v102, v103\n"
"v_cvt_pk_bf16_f32 v79, v104, v105\n"
"ds_write_b128 v56, v[76:79] offset:1024\n"
"v_pk_fma_f32 v[66:67], v[106:107], v[106:107], v[66:67]\n"
"v_pk_fma_f32 v[62:63], v[108:109], v[108:109], v[62:63]\n"
"v_pk_fma_f32 v[66:67], v[110:111], v[110:111], v[66:67]\n"
"v_pk_fma_f32 v[62:63], v[112:113], v[112:113], v[62:63]\n"
"v_pk_fma_f32 v[66:67], v[114:115], v[114:115], v[66:67]\n"
"v_pk_fma_f32 v[62:63], v[116:117], v[116:117], v[62:63]\n"
"v_pk_fma_f32 v[106:107], v[106:107], v[58:59], v[64:65] op_sel_hi:[1,0,0]\n"
"v_pk_fma_f32 v[108:109], v[108:109], v[58:59], v[64:65] op_sel_hi:[1,0,0]\n"
"v_pk_fma_f32 v[110:111], v[110:111], v[58:59], v[64:65] op_sel_hi:[1,0,0]\n"
"v_pk_fma_f32 v[112:113], v[112:113], v[58:59], v[64:65] op_sel_hi:[1,0,0]\n"
"v_pk_fma_f32 v[66:67], v[118:119], v[118:119], v[66:67]\n"
"v_pk_fma_f32 v[62:63], v[120:121], v[120:121], v[62:63]\n"
"v_cvt_pk_bf16_f32 v80, v106, v107\n"
"v_cvt_pk_bf16_f32 v81, v108, v109\n"
"v_cvt_pk_bf16_f32 v82, v110, v111\n"
"v_cvt_pk_bf16_f32 v83, v112, v113\n"
"v_pk_fma_f32 v[66:67], v[122:123], v[122:123], v[66:67]\n"
"v_pk_fma_f32 v[62:63], v[124:125], v[124:125], v[62:63]\n"
"ds_write_b128 v56, v[80:83] offset:2048\n"
"v_pk_add_f32 v[66:67], v[66:67], v[62:63]\n"
"v_add_f32 v66, v66, v67\n"
"v_pk_fma_f32 v[114:115], v[114:115], v[58:59], v[64:65] op_sel_hi:[1,0,0]\n"
"v_pk_fma_f32 v[116:117], v[116:117], v[58:59], v[64:65] op_sel_hi:[1,0,0]\n"
"v_mfma_f32_16x16x4_f32 v[28:31], v234, v66, 0\n"
"v_cvt_pk_bf16_f32 v84, v114, v115\n"
"v_cvt_pk_bf16_f32 v85, v116, v117\n"
"v_pk_fma_f32 v[118:119], v[118:119], v[58:59], v[64:65] op_sel_hi:[1,0,0]\n"
"v_pk_fma_f32 v[120:121], v[120:121], v[58:59], v[64:65] op_sel_hi:[1,0,0]\n"
"v_cvt_pk_bf16_f32 v86, v118, v119\n"
"v_cvt_pk_bf16_f32 v87, v120, v121\n"
"ds_write_b128 v56, v[84:87] offset:3072\n"
"v_pk_fma_f32 v[124:125], v[124:125], v[58:59], v[64:65] op_sel_hi:[1,0,0]\n"
"v_cvt_pk_bf16_f32 v89, v124, v125\n"
"v_pk_fma_f32 v[122:123], v[122:123], v[58:59], v[64:65] op_sel_hi:[1,0,0]\n"
"v_and_b32 v69, s82, v89\n"
"v_cvt_pk_bf16_f32 v88, v122, v123\n"
"v_cndmask_b32 v89, v89, v69, s[56:57]\n"
"v_cmp_nge_f32 vcc, 0x4591425e, v28\n"
"ds_write_b64 v57, v[88:89] offset:4096\n"
"s_nop 0\n"
"s_or_b32 s50, vcc_lo, vcc_hi\n"
"v_mov_b32 v71, s50\n"
"ds_write_b32 v70, v71\n"
"ds_write_b32 v54, v55\n"
"s_waitcnt lgkmcnt(0)\n"
"s_barrier\n"
"v_mov_b32 v67, 122112\n"
"ds_read_b128 v[64:67], v67\n"
"s_waitcnt vmcnt(0)\n"
"v_cmp_eq_u32 vcc, s49, v20\n"
"s_nop 1\n"
"s_and_b32 s48, vcc_lo, vcc_hi\n"
"s_andn2_b32 s50, 7, s48\n"
"s_cmp_eq_u32 s50, 0\n"
"s_cbranch_scc1 L_p0ok_%=\n"
"L_p0sp_%=:\n"
"global_load_dword v20, v19, s[66:67] sc1\n"
"s_waitcnt vmcnt(0)\n"
"v_cmp_eq_u32 vcc, s49, v20\n"
"s_nop 1\n"
"s_and_b32 s48, vcc_lo, vcc_hi\n"
"s_sub_u32 s53, s53, 1\n"
"s_cselect_b32 s50, -1, 0\n"
"s_or_b32 s48, s48, s50\n"
"s_max_i32 s53, s53, 0\n"
"s_andn2_b32 s50, 7, s48\n"
"s_cmp_eq_u32 s50, 0\n"
"s_cbranch_scc0 L_p0sp_%=\n"
"L_p0ok_%=:\n"
"s_movk_i32 s55, 9216\n"
"s_movk_i32 s44, 18432\n"
"s_waitcnt lgkmcnt(0)\n"
"v_or3_b32 v64, v64, v65, v66\n"
"v_or_b32 v64, v64, v67\n"
"s_nop 0\n"
"v_readfirstlane_b32 s50, v64\n"
"s_nop 1\n"
"s_cmp_eq_u32 s50, 0\n"
"s_cbranch_scc0 L_fb_%=\n"
"L_fbret_%=:\n"
"s_add_u32 m0, s85, 0\n"
"s_nop 0\n"
"buffer_load_dwordx4 v10, s[40:43], 0 offen offset:0 sc1 lds\n"
"buffer_load_dwordx4 v11, s[40:43], 0 offen offset:1024 sc1 lds\n"
"v_add_u32 v62, 122368, v238\n"
"v_add_u32 v64, s23, v250\n"
"ds_read_b128 a[0:3], v62 offset:0\n"
"ds_read_b128 a[20:23], v62 offset:4608\n"
"ds_read_b128 a[40:43], v62 offset:9216\n"
"ds_read_b64_tr_b16 a[156:157], v64 offset:0\n"
"ds_read_b64_tr_b16 a[158:159], v64 offset:1024\n"
"s_min_u32 s51, s77, 2\n"
"buffer_load_dwordx4 v12, s[40:43], 0 offen offset:2048 sc1 lds\n"
"s_mul_i32 s51, s51, 4608\n"
"s_cmp_eq_u32 s77, 0\n"
"s_cselect_b64 s[16:17], -1, 0\n"
"s_lshl_b32 s50, s77, 3\n"
"s_add_u32 s50, s50, 119808\n"
"s_mul_i32 s96, s77, 10240\n"
"s_add_u32 s56, s64, 5111808\n"
"s_addc_u32 s57, s65, 0\n"
"s_add_u32 s97, s96, 5120\n"
"s_mov_b32 s56, s56\n"
"s_and_b32 s57, s57, 0xffff\n"
"s_mov_b32 s58, 92160\n"
"s_mov_b32 s59, 0x20000\n"
"buffer_load_dwordx4 v13, s[40:43], 0 offen offset:3072 sc1 lds\n"
"s_add_u32 m0, s85, 4096\n"
"s_add_u32 s86, s96, 46080\n"
"s_add_u32 s87, s97, 46080\n"
"ds_read_b128 a[4:7], v62 offset:1024\n"
"ds_read_b128 a[24:27], v62 offset:5632\n"
"ds_read_b128 a[44:47], v62 offset:10240\n"
"s_waitcnt lgkmcnt(3)\n"
"v_mfma_f32_16x16x32_bf16 v[72:75], a[156:159], a[0:3], 0\n"
"ds_read_b64_tr_b16 a[160:161], v64 offset:2048\n"
"ds_read_b64_tr_b16 a[162:163], v64 offset:3072\n"
"v_mfma_f32_16x16x32_bf16 v[76:79], a[156:159], a[20:23], 0\n"
"ds_read_b128 a[8:11], v62 offset:2048\n"
"ds_read_b128 a[28:31], v62 offset:6656\n"
"buffer_load_dwordx4 v14, s[40:43], 0 offen offset:0 sc1 lds\n"
"v_mfma_f32_16x16x32_bf16 v[80:83], a[156:159], a[40:43], 0\n"
"ds_read_b128 a[48:51], v62 offset:11264\n"
"ds_read_b64_tr_b16 a[164:165], v64 offset:4096\n"
"ds_read_b64_tr_b16 a[166:167], v64 offset:5120\n"
"ds_read_b128 a[12:15], v62 offset:3072\n"
"v_mov_b64 v[216:217], -1\n"
"v_mov_b64 v[218:219], -1\n"
"buffer_load_dwordx4 v15, s[40:43], 0 offen offset:1024 sc1 lds\n"
"ds_read_b128 a[32:35], v62 offset:7680\n"
"ds_read_b128 a[52:55], v62 offset:12288\n"
"v_add_u32 v63, 122368, v23\n"
"v_add_u32 v60, s51, v62\n"
"v_add_u32 v66, 154624, v251\n"
"s_waitcnt lgkmcnt(8)\n"
"v_mfma_f32_16x16x32_bf16 v[72:75], a[160:163], a[4:7], v[72:75]\n"
"ds_read_b64_tr_b16 a[168:169], v64 offset:6144\n"
"ds_read_b64_tr_b16 a[170:171], v64 offset:7168\n"
"v_mfma_f32_16x16x32_bf16 v[76:79], a[160:163], a[24:27], v[76:79]\n"
"ds_read_b64 a[16:17], v63 offset:4096\n"
"ds_read_b64 a[36:37], v63 offset:8704\n"
"buffer_load_dwordx4 v16, s[40:43], 0 offen offset:2048 sc1 lds\n"
"v_mfma_f32_16x16x32_bf16 v[80:83], a[160:163], a[44:47], v[80:83]\n"
"ds_read_b64 a[56:57], v63 offset:13312\n"
"ds_read_b64_tr_b16 a[188:189], v64 offset:8192\n"
"s_waitcnt lgkmcnt(9)\n"
"v_mfma_f32_16x16x32_bf16 v[72:75], a[164:167], a[8:11], v[72:75]\n"
"ds_read_b128 v[40:43], v60 offset:0\n"
"ds_read_b64_tr_b16 a[156:157], v66 offset:0\n"
"v_mfma_f32_16x16x32_bf16 v[76:79], a[164:167], a[28:31], v[76:79]\n"
"ds_read_b64_tr_b16 a[158:159], v66 offset:1024\n"
"ds_read_b128 v[44:47], v60 offset:1024\n"
"v_mfma_f32_16x16x32_bf16 v[80:83], a[164:167], a[48:51], v[80:83]\n"
"buffer_load_dwordx4 v17, s[40:43], 0 offen offset:3072 sc1 lds\n"
"s_add_u32 m0, s85, 8192\n"
"ds_read_b64_tr_b16 a[160:161], v66 offset:2048\n"
"ds_read_b64_tr_b16 a[162:163], v66 offset:3072\n"
"s_waitcnt lgkmcnt(10)\n"
"v_mfma_f32_16x16x32_bf16 v[72:75], a[168:171], a[12:15], v[72:75]\n"
"ds_read_b128 v[48:51], v60 offset:2048\n"
"ds_read_b64_tr_b16 a[164:165], v66 offset:4096\n"
"v_mfma_f32_16x16x32_bf16 v[76:79], a[168:171], a[32:35], v[76:79]\n"
"ds_read_b64_tr_b16 a[166:167], v66 offset:5120\n"
"ds_read_b128 v[52:55], v60 offset:3072\n"
"v_mfma_f32_16x16x32_bf16 v[80:83], a[168:171], a[52:55], v[80:83]\n"
"ds_read_b64_tr_b16 a[168:169], v66 offset:6144\n"
"v_add_u32 v65, s23, v251\n"
"buffer_load_dwordx4 v18, s[40:43], 0 offen offset:0 sc1 lds\n"
"s_add_u32 m0, s20, 0\n"
"s_waitcnt lgkmcnt(11)\n"
"v_mfma_f32_16x16x32_bf16 v[72:75], a[188:191], a[16:19], v[72:75]\n"
"ds_read_b64_tr_b16 a[172:173], v65 offset:0\n"
"ds_read_b64_tr_b16 a[174:175], v65 offset:1024\n"
"v_mfma_f32_16x16x32_bf16 v[76:79], a[188:191], a[36:39], v[76:79]\n"
"ds_read_b64_tr_b16 a[176:177], v65 offset:2048\n"
"ds_read_b64_tr_b16 a[178:179], v65 offset:3072\n"
"ds_read_b64_tr_b16 a[180:181], v65 offset:4096\n"
"ds_read_b64_tr_b16 a[182:183], v65 offset:5120\n"
"buffer_load_dwordx4 v10, s[40:43], s55 offen offset:0 sc1 lds\n"
"s_waitcnt lgkmcnt(14)\n"
"v_mfma_f32_16x16x32_bf16 v[96:99], a[156:159], v[40:43], 0\n"
"ds_read_b64_tr_b16 a[170:171], v66 offset:7168\n"
"ds_read_b64_tr_b16 a[184:185], v65 offset:6144\n"
"v_accvgpr_write_b32 a58, 0\n"
"v_accvgpr_write_b32 a59, 0\n"
"v_add_u32 v61, s51, v63\n"
"s_nop 0\n"
"v_mfma_f32_16x16x32_bf16 v[80:83], a[188:191], a[56:59], v[80:83]\n"
"s_lshl_b32 s51, s77, 10\n"
"s_add_u32 s51, s51, 110592\n"
"ds_read_b64_tr_b16 a[186:187], v65 offset:7168\n"
"s_waitcnt lgkmcnt(14)\n"
"v_mfma_f32_16x16x32_bf16 v[96:99], a[160:163], v[44:47], v[96:99]\n"
"buffer_load_dwordx4 v11, s[40:43], s55 offen offset:1024 sc1 lds\n"
"ds_read_b64 v[56:57], v61 offset:4096\n"
"s_waitcnt lgkmcnt(8)\n"
"v_mfma_f32_16x16x32_bf16 v[84:87], a[172:175], a[0:3], 0\n"
"ds_read_b64_tr_b16 a[188:189], v66 offset:8192\n"
"ds_read_b64_tr_b16 a[192:193], v65 offset:8192\n"
"v_mfma_f32_16x16x32_bf16 v[88:91], a[172:175], a[20:23], 0\n"
"v_mov_b64 v[58:59], 0\n"
"v_add_u32 v67, s51, v238\n"
"s_waitcnt lgkmcnt(8)\n"
"v_mfma_f32_16x16x32_bf16 v[84:87], a[176:179], a[4:7], v[84:87]\n"
"s_lshr_b32 s51, s77, 1\n"
"s_lshl_b32 s51, s51, 20\n"
"s_add_u32 s98, s51, 40960\n"
"buffer_load_dwordx4 v12, s[40:43], s55 offen offset:2048 sc1 lds\n"
"v_mfma_f32_16x16x32_bf16 v[96:99], a[164:167], v[48:51], v[96:99]\n"
"s_lshl1_add_u32 s51, s77, s76\n"
"s_add_u32 s52, s98, 46080\n"
"s_cmp_eq_u32 s51, 5\n"
"s_cselect_b32 s35, 2, 0\n"
"v_mfma_f32_16x16x32_bf16 v[92:95], a[172:175], a[40:43], 0\n"
"v_lshl_add_u32 v1, v21, 5, s50\n"
"s_sub_u32 s50, s85, s45\n"
"v_mfma_f32_16x16x32_bf16 v[88:91], a[176:179], a[24:27], v[88:91]\n"
"v_add_u32 v138, 136192, v10\n"
"v_add_u32 v226, s50, v2\n"
"buffer_load_dwordx4 v13, s[40:43], s55 offen offset:3072 sc1 lds\n"
"s_add_u32 m0, s20, 4096\n"
"s_waitcnt lgkmcnt(6)\n"
"v_mfma_f32_16x16x32_bf16 v[84:87], a[180:183], a[8:11], v[84:87]\n"
"ds_read_b128 a[76:79], v138 offset:0\n"
"ds_read_b128 a[80:83], v138 offset:1024\n"
"s_waitcnt lgkmcnt(7)\n"
"v_mfma_f32_16x16x32_bf16 v[96:99], a[168:171], v[52:55], v[96:99]\n"
"ds_read_b128 a[84:87], v138 offset:2048\n"
"ds_read_b128 a[88:91], v138 offset:3072\n"
"v_mfma_f32_16x16x32_bf16 v[92:95], a[176:179], a[44:47], v[92:95]\n"
"ds_read_b128 a[96:99], v138 offset:4608\n"
"v_add_u32 v139, 136192, v23\n"
"buffer_load_dwordx4 v14, s[40:43], s55 offen offset:0 sc1 lds\n"
"v_mfma_f32_16x16x32_bf16 v[88:91], a[180:183], a[28:31], v[88:91]\n"
"ds_read_b128 a[100:103], v138 offset:5632\n"
"ds_read_b128 a[104:107], v138 offset:6656\n"
"s_waitcnt lgkmcnt(10)\n"
"v_mfma_f32_16x16x32_bf16 v[84:87], a[184:187], a[12:15], v[84:87]\n"
"ds_read_b128 a[108:111], v138 offset:7680\n"
"v_add_u32 v227, s50, v3\n"
"s_waitcnt lgkmcnt(9)\n"
"v_mfma_f32_16x16x32_bf16 v[96:99], a[188:191], v[56:59], v[96:99]\n"
"ds_read_b128 a[116:119], v138 offset:9216\n"
"v_and_b32 v222, 16, v0\n"
"buffer_load_dwordx4 v15, s[40:43], s55 offen offset:1024 sc1 lds\n"
"v_mfma_f32_16x16x32_bf16 v[92:95], a[180:183], a[48:51], v[92:95]\n"
"ds_read_b128 a[120:123], v138 offset:10240\n"
"ds_read_b128 a[124:127], v138 offset:11264\n"
"v_mfma_f32_16x16x32_bf16 v[88:91], a[184:187], a[32:35], v[88:91]\n"
"ds_read_b128 a[128:131], v138 offset:12288\n"
"v_lshrrev_b32 v239, 5, v0\n"
"s_waitcnt lgkmcnt(12)\n"
"v_mfma_f32_16x16x32_bf16 v[84:87], a[192:195], a[16:19], v[84:87]\n"
"ds_read_b128 a[136:139], v138 offset:13824\n"
"v_lshlrev_b32 v222, 1, v222\n"
"v_mfma_f32_16x16x32_bf16 v[92:95], a[184:187], a[52:55], v[92:95]\n"
"buffer_load_dwordx4 v16, s[40:43], s55 offen offset:2048 sc1 lds\n"
"v_mfma_f32_16x16x32_bf16 v[88:91], a[192:195], a[36:39], v[88:91]\n"
"ds_read_b128 a[140:143], v138 offset:14848\n"
"ds_read_b128 a[144:147], v138 offset:15872\n"
"v_mfma_f32_16x16x32_bf16 v[92:95], a[192:195], a[56:59], v[92:95]\n"
"ds_read_b128 a[148:151], v138 offset:16896\n"
"v_lshl_add_u32 v240, v239, 4, v222\n"
"ds_read_b64 a[92:93], v139 offset:4096\n"
"v_pk_mul_f32 v[108:109], v[72:73], v[72:73]\n"
"buffer_load_dwordx4 v17, s[40:43], s55 offen offset:3072 sc1 lds\n"
"s_add_u32 m0, s20, 8192\n"
"v_pk_mul_f32 v[140:141], v[96:97], v[96:97]\n"
"v_pk_mul_f32 v[142:143], v[98:99], v[98:99]\n"
"v_pk_mul_f32 v[110:111], v[74:75], v[74:75]\n"
"v_pk_fma_f32 v[108:109], v[108:109], s[92:93], v[236:237] op_sel_hi:[1,1,0]\n"
"v_pk_fma_f32 v[140:141], v[140:141], s[92:93], v[236:237] op_sel_hi:[1,1,0]\n"
"v_pk_fma_f32 v[142:143], v[142:143], s[92:93], v[236:237] op_sel_hi:[1,1,0]\n"
"v_pk_fma_f32 v[110:111], v[110:111], s[92:93], v[236:237] op_sel_hi:[1,1,0]\n"
"v_pk_mul_f32 v[108:109], v[108:109], v[72:73]\n"
"v_pk_mul_f32 v[100:101], v[84:85], v[84:85]\n"
"v_pk_mul_f32 v[140:141], v[140:141], v[96:97]\n"
"v_pk_mul_f32 v[142:143], v[142:143], v[98:99]\n"
"buffer_load_dwordx4 v18, s[40:43], s55 offen offset:0 sc1 lds\n"
"v_pk_mul_f32 v[110:111], v[110:111], v[74:75]\n"
"v_exp_f32 v108, v108\n"
"v_exp_f32 v109, v109\n"
"v_pk_mul_f32 v[102:103], v[86:87], v[86:87]\n"
"v_pk_fma_f32 v[100:101], v[100:101], s[92:93], v[236:237] op_sel_hi:[1,1,0]\n"
"v_exp_f32 v140, v140\n"
"v_exp_f32 v141, v141\n"
"v_exp_f32 v142, v142\n"
"global_store_dwordx4 v238, v[216:219], s[70:71] sc1\n"
"v_exp_f32 v143, v143\n"
"v_pk_mul_f32 v[112:113], v[76:77], v[76:77]\n"
"v_exp_f32 v110, v110\n"
"v_exp_f32 v111, v111\n"
"v_pk_fma_f32 v[102:103], v[102:103], s[92:93], v[236:237] op_sel_hi:[1,1,0]\n"
"v_pk_mul_f32 v[100:101], v[100:101], v[84:85]\n"
"v_pk_mul_f32 v[114:115], v[78:79], v[78:79]\n"
"v_pk_fma_f32 v[112:113], v[112:113], s[92:93], v[236:237] op_sel_hi:[1,1,0]\n"
"v_pk_mul_f32 v[102:103], v[102:103], v[86:87]\n"
"global_store_dwordx4 v238, v[216:219], s[70:71] offset:1024 sc1\n"
"v_exp_f32 v100, v100\n"
"v_exp_f32 v101, v101\n"
"v_pk_fma_f32 v[114:115], v[114:115], s[92:93], v[236:237] op_sel_hi:[1,1,0]\n"
"v_pk_mul_f32 v[112:113], v[112:113], v[76:77]\n"
"v_pk_add_f32 v[108:109], v[108:109], v[234:235] op_sel_hi:[1,0]\n"
"v_pk_mul_f32 v[104:105], v[88:89], v[88:89]\n"
"v_exp_f32 v102, v102\n"
"v_exp_f32 v103, v103\n"
"global_store_dwordx2 v23, v[216:217], s[70:71] offset:2048 sc1\n"
"v_pk_add_f32 v[140:141], v[140:141], v[234:235] op_sel_hi:[1,0]\n"
"v_pk_add_f32 v[142:143], v[142:143], v[234:235] op_sel_hi:[1,0]\n"
"v_pk_mul_f32 v[114:115], v[114:115], v[78:79]\n"
"v_exp_f32 v112, v112\n"
"v_exp_f32 v113, v113\n"
"v_pk_add_f32 v[110:111], v[110:111], v[234:235] op_sel_hi:[1,0]\n"
"v_rcp_f32 v108, v108\n"
"v_rcp_f32 v109, v109\n"
"v_pk_mul_f32 v[106:107], v[90:91], v[90:91]\n"
"v_pk_fma_f32 v[104:105], v[104:105], s[92:93], v[236:237] op_sel_hi:[1,1,0]\n"
"v_rcp_f32 v140, v140\n"
"v_rcp_f32 v141, v141\n"
"v_rcp_f32 v142, v142\n"
"v_rcp_f32 v143, v143\n"
"v_pk_mul_f32 v[116:117], v[80:81], v[80:81]\n"
"v_exp_f32 v114, v114\n"
"v_exp_f32 v115, v115\n"
"v_rcp_f32 v110, v110\n"
"v_rcp_f32 v111, v111\n"
"v_pk_fma_f32 v[106:107], v[106:107], s[92:93], v[236:237] op_sel_hi:[1,1,0]\n"
"v_pk_mul_f32 v[104:105], v[104:105], v[88:89]\n"
"v_pk_add_f32 v[100:101], v[100:101], v[234:235] op_sel_hi:[1,0]\n"
"v_pk_mul_f32 v[118:119], v[82:83], v[82:83]\n"
"v_pk_fma_f32 v[116:117], v[116:117], s[92:93], v[236:237] op_sel_hi:[1,1,0]\n"
"v_pk_mul_f32 v[106:107], v[106:107], v[90:91]\n"
"v_exp_f32 v104, v104\n"
"v_exp_f32 v105, v105\n"
"v_pk_add_f32 v[102:103], v[102:103], v[234:235] op_sel_hi:[1,0]\n"
"v_rcp_f32 v100, v100\n"
"v_rcp_f32 v101, v101\n"
"v_pk_fma_f32 v[118:119], v[118:119], s[92:93], v[236:237] op_sel_hi:[1,1,0]\n"
"v_pk_mul_f32 v[116:117], v[116:117], v[80:81]\n"
"v_pk_add_f32 v[112:113], v[112:113], v[234:235] op_sel_hi:[1,0]\n"
"v_pk_mul_f32 v[72:73], v[72:73], v[108:109]\n"
"v_pk_mul_f32 v[68:69], v[92:93], v[92:93]\n"
"v_exp_f32 v106, v106\n"
"v_exp_f32 v107, v107\n"
"v_rcp_f32 v102, v102\n"
"v_rcp_f32 v103, v103\n"
"v_pk_mul_f32 v[96:97], v[96:97], v[140:141]\n"
"v_pk_mul_f32 v[98:99], v[98:99], v[142:143]\n"
"v_pk_mul_f32 v[118:119], v[118:119], v[82:83]\n"
"v_exp_f32 v116, v116\n"
"v_exp_f32 v117, v117\n"
"v_pk_add_f32 v[114:115], v[114:115], v[234:235] op_sel_hi:[1,0]\n"
"v_rcp_f32 v112, v112\n"
"v_rcp_f32 v113, v113\n"
"v_pk_mul_f32 v[74:75], v[74:75], v[110:111]\n"
"v_pk_mul_f32 v[70:71], v[94:95], v[94:95]\n"
"v_pk_fma_f32 v[68:69], v[68:69], s[92:93], v[236:237] op_sel_hi:[1,1,0]\n"
"v_pk_mul_f32 v[122:123], v[72:73], v[72:73]\n"
"v_pk_add_f32 v[140:141], v[96:97], v[98:99]\n"
"v_pk_mul_f32 v[142:143], v[96:97], v[96:97]\n"
"v_exp_f32 v118, v118\n"
"v_exp_f32 v119, v119\n"
"v_rcp_f32 v114, v114\n"
"v_rcp_f32 v115, v115\n"
"v_pk_fma_f32 v[70:71], v[70:71], s[92:93], v[236:237] op_sel_hi:[1,1,0]\n"
"v_pk_mul_f32 v[68:69], v[68:69], v[92:93]\n"
"v_pk_add_f32 v[104:105], v[104:105], v[234:235] op_sel_hi:[1,0]\n"
"v_pk_mul_f32 v[84:85], v[84:85], v[100:101]\n"
"v_pk_add_f32 v[120:121], v[72:73], v[74:75]\n"
"v_pk_fma_f32 v[122:123], v[74:75], v[74:75], v[122:123]\n"
"v_pk_fma_f32 v[142:143], v[98:99], v[98:99], v[142:143]\n"
"v_add_f32 v140, v140, v141\n"
"v_pk_mul_f32 v[70:71], v[70:71], v[94:95]\n"
"v_exp_f32 v68, v68\n"
"v_exp_f32 v69, v69\n"
"v_pk_add_f32 v[106:107], v[106:107], v[234:235] op_sel_hi:[1,0]\n"
"v_rcp_f32 v104, v104\n"
"v_rcp_f32 v105, v105\n"
"v_pk_mul_f32 v[86:87], v[86:87], v[102:103]\n"
"v_pk_add_f32 v[120:121], v[120:121], v[84:85]\n"
"v_pk_fma_f32 v[122:123], v[84:85], v[84:85], v[122:123]\n"
"v_add_f32 v141, v142, v143\n"
"v_pk_add_f32 v[116:117], v[116:117], v[234:235] op_sel_hi:[1,0]\n"
"v_pk_mul_f32 v[76:77], v[76:77], v[112:113]\n"
"v_exp_f32 v70, v70\n"
"v_exp_f32 v71, v71\n"
"v_rcp_f32 v106, v106\n"
"v_rcp_f32 v107, v107\n"
"v_pk_add_f32 v[120:121], v[120:121], v[86:87]\n"
"v_pk_fma_f32 v[122:123], v[86:87], v[86:87], v[122:123]\n"
"v_pk_add_f32 v[118:119], v[118:119], v[234:235] op_sel_hi:[1,0]\n"
"v_rcp_f32 v116, v116\n"
"v_rcp_f32 v117, v117\n"
"v_pk_mul_f32 v[78:79], v[78:79], v[114:115]\n"
"v_add_f32 v132, v120, v121\n"
"v_add_f32 v133, v122, v123\n"
"v_cndmask_b32 v142, 0, v140, s[16:17]\n"
"v_cndmask_b32 v143, 0, v141, s[16:17]\n"
"v_pk_mul_f32 v[126:127], v[76:77], v[76:77]\n"
"v_rcp_f32 v118, v118\n"
"v_rcp_f32 v119, v119\n"
"v_pk_add_f32 v[68:69], v[68:69], v[234:235] op_sel_hi:[1,0]\n"
"v_pk_mul_f32 v[88:89], v[88:89], v[104:105]\n"
"v_pk_add_f32 v[132:133], v[132:133], v[142:143]\n"
"s_cmp_eq_u32 s77, 1\n"
"s_cselect_b64 s[16:17], -1, 0\n"
"v_mfma_f32_16x16x4_f32 v[24:27], v234, v132, 0\n"
"s_waitcnt vmcnt(12)\n"
"ds_write_b128 v67, v[96:99]\n"
"v_rcp_f32 v68, v68\n"
"v_rcp_f32 v69, v69\n"
"v_cndmask_b32 v142, 0, v140, s[16:17]\n"
"v_mfma_f32_16x16x4_f32 v[28:31], v234, v133, 0\n"
"v_cndmask_b32 v143, 0, v141, s[16:17]\n"
"s_cmp_eq_u32 s77, 2\n"
"s_cselect_b64 s[16:17], -1, 0\n"
"ds_read_b128 a[0:3], v226 offset:0\n"
"ds_read_b128 a[4:7], v226 offset:2048\n"
"ds_read_b128 a[8:11], v226 offset:4096\n"
"ds_read_b128 a[12:15], v226 offset:6144\n"
"v_pk_add_f32 v[124:125], v[76:77], v[78:79]\n"
"v_pk_fma_f32 v[126:127], v[78:79], v[78:79], v[126:127]\n"
"v_pk_add_f32 v[70:71], v[70:71], v[234:235] op_sel_hi:[1,0]\n"
"v_pk_mul_f32 v[90:91], v[90:91], v[106:107]\n"
"v_pk_add_f32 v[124:125], v[124:125], v[88:89]\n"
"v_pk_fma_f32 v[126:127], v[88:89], v[88:89], v[126:127]\n"
"v_pk_mul_f32 v[80:81], v[80:81], v[116:117]\n"
"v_rcp_f32 v70, v70\n"
"v_rcp_f32 v71, v71\n"
"v_pk_add_f32 v[124:125], v[124:125], v[90:91]\n"
"v_pk_fma_f32 v[126:127], v[90:91], v[90:91], v[126:127]\n"
"v_pk_mul_f32 v[82:83], v[82:83], v[118:119]\n"
"v_add_f32 v134, v124, v125\n"
"v_add_f32 v135, v126, v127\n"
"v_pk_mul_f32 v[130:131], v[80:81], v[80:81]\n"
"v_pk_mul_f32 v[92:93], v[92:93], v[68:69]\n"
"v_pk_add_f32 v[134:135], v[134:135], v[142:143]\n"
"v_pk_add_f32 v[128:129], v[80:81], v[82:83]\n"
"v_pk_fma_f32 v[130:131], v[82:83], v[82:83], v[130:131]\n"
"v_mfma_f32_16x16x4_f32 v[32:35], v234, v134, 0\n"
"ds_read_b128 a[20:23], v226 offset:512\n"
"ds_read_b128 a[24:27], v226 offset:2560\n"
"ds_read_b128 a[28:31], v226 offset:4608\n"
"ds_read_b128 a[32:35], v226 offset:6656\n"
"v_cndmask_b32 v142, 0, v140, s[16:17]\n"
"v_cndmask_b32 v143, 0, v141, s[16:17]\n"
"v_mfma_f32_16x16x4_f32 v[36:39], v234, v135, 0\n"
"v_mov_b32 v25, v28\n"
"ds_write_b64 v1, v[24:25] offset:0\n"
"ds_read_b64 a[112:113], v139 offset:8704\n"
"ds_read_b64 a[132:133], v139 offset:13312\n"
"ds_read_b64 a[152:153], v139 offset:17920\n"
"v_pk_mul_f32 v[94:95], v[94:95], v[70:71]\n"
"v_pk_add_f32 v[128:129], v[128:129], v[92:93]\n"
"v_pk_fma_f32 v[130:131], v[92:93], v[92:93], v[130:131]\n"
"v_pk_add_f32 v[128:129], v[128:129], v[94:95]\n"
"v_pk_fma_f32 v[130:131], v[94:95], v[94:95], v[130:131]\n"
"v_add_f32 v136, v128, v129\n"
"v_add_f32 v137, v130, v131\n"
"v_pk_add_f32 v[136:137], v[136:137], v[142:143]\n"
"v_mov_b32 v33, v36\n"
"v_accvgpr_write_b32 a206, v72\n"
"v_mfma_f32_16x16x4_f32 v[108:111], v234, v136, 0\n"
"ds_write_b64 v1, v[32:33] offset:512\n"
"ds_read_b64 a[16:17], v227 offset:0\n"
"ds_read_b64 a[36:37], v227 offset:512\n"
"v_accvgpr_write_b32 a207, v73\n"
"v_accvgpr_write_b32 a208, v74\n"
"v_accvgpr_write_b32 a209, v75\n"
"v_mfma_f32_16x16x4_f32 v[112:115], v234, v137, 0\n"
"v_accvgpr_write_b32 a210, v76\n"
"v_accvgpr_write_b32 a211, v77\n"
"v_accvgpr_write_b32 a212, v78\n"
"v_accvgpr_write_b32 a213, v79\n"
"v_accvgpr_write_b32 a214, v80\n"
"v_accvgpr_write_b32 a215, v81\n"
"v_accvgpr_write_b32 a216, v82\n"
"v_accvgpr_write_b32 a217, v83\n"
"v_accvgpr_write_b32 a218, v84\n"
"v_accvgpr_write_b32 a219, v85\n"
"v_accvgpr_write_b32 a220, v86\n"
"v_accvgpr_write_b32 a221, v87\n"
"v_accvgpr_write_b32 a222, v88\n"
"v_mov_b32 v109, v112\n"
"ds_write_b64 v1, v[108:109] offset:1024\n"
"v_accvgpr_write_b32 a223, v89\n"
"v_accvgpr_write_b32 a224, v90\n"
"v_accvgpr_write_b32 a225, v91\n"
"v_accvgpr_write_b32 a226, v92\n"
"v_accvgpr_write_b32 a227, v93\n"
"v_accvgpr_write_b32 a228, v94\n"
"v_accvgpr_write_b32 a229, v95\n"
"v_lshlrev_b32 v239, 3, v22\n"
"v_mov_b64 v[216:217], -1\n"
"v_mov_b64 v[218:219], -1\n"
"v_mov_b32 v220, s75\n"
"v_mov_b32 v221, s49\n"
"v_mov_b32 v64, 122128\n"
"v_mov_b32 v65, 1\n"
"s_waitcnt lgkmcnt(0)\n"
"ds_add_u32 v64, v65\n"
"s_cmp_eq_u32 s79, 16\n"
"s_waitcnt vmcnt(12)\n"
"s_cbranch_scc0 L_first1_%=\n"
"v_mfma_f32_16x16x32_bf16 v[24:27], a[0:3], a[76:79], 0\n"
"global_load_dword v20, v19, s[66:67] sc1\n"
"v_mfma_f32_16x16x32_bf16 v[24:27], a[4:7], a[80:83], v[24:27]\n"
"s_add_u32 m0, s45, 36864\n"
"ds_read_b64_tr_b16 a[40:41], v4 offset:0\n"
"v_mfma_f32_16x16x32_bf16 v[24:27], a[8:11], a[84:87], v[24:27]\n"
"buffer_load_dwordx4 v10, s[40:43], s44 offen offset:0 sc1 lds\n"
"v_mfma_f32_16x16x32_bf16 v[24:27], a[12:15], a[88:91], v[24:27]\n"
"ds_read_b64_tr_b16 a[42:43], v4 offset:512\n"
"v_mfma_f32_16x16x32_bf16 v[24:27], a[16:19], a[92:95], v[24:27]\n"
"ds_read_b64_tr_b16 a[44:45], v4 offset:1024\n"
"v_mfma_f32_16x16x32_bf16 v[28:31], a[20:23], a[76:79], 0\n"
"buffer_load_dwordx4 v11, s[40:43], s44 offen offset:1024 sc1 lds\n"
"v_mfma_f32_16x16x32_bf16 v[28:31], a[24:27], a[80:83], v[28:31]\n"
"ds_read_b64_tr_b16 a[46:47], v4 offset:1536\n"
"v_mfma_f32_16x16x32_bf16 v[28:31], a[28:31], a[84:87], v[28:31]\n"
"v_mfma_f32_16x16x32_bf16 v[28:31], a[32:35], a[88:91], v[28:31]\n"
"ds_read_b64_tr_b16 a[48:49], v4 offset:2048\n"
"v_mfma_f32_16x16x32_bf16 v[28:31], a[36:39], a[92:95], v[28:31]\n"
"buffer_load_dwordx4 v12, s[40:43], s44 offen offset:2048 sc1 lds\n"
"v_mfma_f32_16x16x32_bf16 v[32:35], a[0:3], a[96:99], 0\n"
"ds_read_b64_tr_b16 a[50:51], v4 offset:2560\n"
"v_mfma_f32_16x16x32_bf16 v[32:35], a[4:7], a[100:103], v[32:35]\n"
"v_exp_f32 v24, v24\n"
"v_mfma_f32_16x16x32_bf16 v[32:35], a[8:11], a[104:107], v[32:35]\n"
"v_exp_f32 v25, v25\n"
"v_mfma_f32_16x16x32_bf16 v[32:35], a[12:15], a[108:111], v[32:35]\n"
"buffer_load_dwordx4 v13, s[40:43], s44 offen offset:3072 sc1 lds\n"
"v_mfma_f32_16x16x32_bf16 v[32:35], a[16:19], a[112:115], v[32:35]\n"
"v_exp_f32 v26, v26\n"
"v_mfma_f32_16x16x32_bf16 v[36:39], a[20:23], a[96:99], 0\n"
"ds_read_b64_tr_b16 a[52:53], v4 offset:3072\n"
"v_cvt_pk_bf16_f32 v56, v24, v25\n"
"v_mfma_f32_16x16x32_bf16 v[36:39], a[24:27], a[100:103], v[36:39]\n"
"s_add_u32 m0, s45, 40960\n"
"v_exp_f32 v27, v27\n"
"v_mfma_f32_16x16x32_bf16 v[36:39], a[28:31], a[104:107], v[36:39]\n"
"buffer_load_dwordx4 v14, s[40:43], s44 offen offset:0 sc1 lds\n"
"v_mfma_f32_16x16x32_bf16 v[36:39], a[32:35], a[108:111], v[36:39]\n"
"v_exp_f32 v28, v28\n"
"v_mfma_f32_16x16x32_bf16 v[36:39], a[36:39], a[112:115], v[36:39]\n"
"ds_read_b64_tr_b16 a[54:55], v4 offset:3584\n"
"v_cvt_pk_bf16_f32 v57, v26, v27\n"
"v_mfma_f32_16x16x32_bf16 v[40:43], a[0:3], a[116:119], 0\n"
"buffer_load_dwordx4 v15, s[40:43], s44 offen offset:1024 sc1 lds\n"
"v_mfma_f32_16x16x32_bf16 v[40:43], a[4:7], a[120:123], v[40:43]\n"
"v_exp_f32 v29, v29\n"
"v_mfma_f32_16x16x32_bf16 v[40:43], a[8:11], a[124:127], v[40:43]\n"
"v_exp_f32 v30, v30\n"
"v_mfma_f32_16x16x32_bf16 v[40:43], a[12:15], a[128:131], v[40:43]\n"
"ds_read_b64_tr_b16 a[56:57], v4 offset:4096\n"
"v_cvt_pk_bf16_f32 v58, v28, v29\n"
"v_mfma_f32_16x16x32_bf16 v[40:43], a[16:19], a[132:135], v[40:43]\n"
"buffer_load_dwordx4 v16, s[40:43], s44 offen offset:2048 sc1 lds\n"
"v_mfma_f32_16x16x32_bf16 v[44:47], a[20:23], a[116:119], 0\n"
"v_exp_f32 v31, v31\n"
"v_mfma_f32_16x16x32_bf16 v[44:47], a[24:27], a[120:123], v[44:47]\n"
"v_exp_f32 v32, v32\n"
"v_mfma_f32_16x16x32_bf16 v[44:47], a[28:31], a[124:127], v[44:47]\n"
"ds_read_b64_tr_b16 a[58:59], v4 offset:4608\n"
"v_cvt_pk_bf16_f32 v59, v30, v31\n"
"v_mfma_f32_16x16x32_bf16 v[44:47], a[32:35], a[128:131], v[44:47]\n"
"buffer_load_dwordx4 v17, s[40:43], s44 offen offset:3072 sc1 lds\n"
"v_mfma_f32_16x16x32_bf16 v[44:47], a[36:39], a[132:135], v[44:47]\n"
"v_exp_f32 v33, v33\n"
"v_mfma_f32_16x16x32_bf16 v[48:51], a[0:3], a[136:139], 0\n"
"v_exp_f32 v34, v34\n"
"v_mfma_f32_16x16x32_bf16 v[48:51], a[4:7], a[140:143], v[48:51]\n"
"s_add_u32 m0, s45, 45056\n"
"ds_read_b64_tr_b16 a[60:61], v4 offset:5120\n"
"v_cvt_pk_bf16_f32 v60, v32, v33\n"
"v_mfma_f32_16x16x32_bf16 v[48:51], a[8:11], a[144:147], v[48:51]\n"
"buffer_load_dwordx4 v18, s[40:43], s44 offen offset:0 sc1 lds\n"
"v_mfma_f32_16x16x32_bf16 v[48:51], a[12:15], a[148:151], v[48:51]\n"
"s_add_u32 s44, s44, 9216\n"
"v_exp_f32 v35, v35\n"
"v_mfma_f32_16x16x32_bf16 v[48:51], a[16:19], a[152:155], v[48:51]\n"
"v_exp_f32 v36, v36\n"
"v_mfma_f32_16x16x32_bf16 v[52:55], a[20:23], a[136:139], 0\n"
"ds_read_b64_tr_b16 a[62:63], v4 offset:5632\n"
"v_cvt_pk_bf16_f32 v61, v34, v35\n"
"v_mfma_f32_16x16x32_bf16 v[52:55], a[24:27], a[140:143], v[52:55]\n"
"v_exp_f32 v37, v37\n"
"v_mfma_f32_16x16x32_bf16 v[52:55], a[28:31], a[144:147], v[52:55]\n"
"v_exp_f32 v38, v38\n"
"v_exp_f32 v40, v40\n"
"v_mfma_f32_16x16x32_bf16 v[52:55], a[32:35], a[148:151], v[52:55]\n"
"ds_read_b64_tr_b16 a[64:65], v4 offset:6144\n"
"v_cvt_pk_bf16_f32 v62, v36, v37\n"
"v_exp_f32 v41, v41\n"
"v_mfma_f32_16x16x32_bf16 v[52:55], a[36:39], a[152:155], v[52:55]\n"
"s_waitcnt vmcnt(9) lgkmcnt(3)\n"
"v_exp_f32 v39, v39\n"
"s_nop 0\n"
"v_cvt_pk_bf16_f32 v63, v38, v39\n"
"v_exp_f32 v42, v42\n"
"v_mfma_f32_16x16x32_bf16 v[72:75], a[40:43], v[56:59], 0\n"
"ds_read_b64_tr_b16 a[66:67], v4 offset:6656\n"
"v_cmp_eq_u32 vcc, s49, v20\n"
"v_cvt_pk_bf16_f32 v64, v40, v41\n"
"v_mfma_f32_16x16x32_bf16 v[108:111], a[40:43], v[60:63], 0\n"
"ds_read_b64_tr_b16 a[68:69], v4 offset:7168\n"
"ds_read_b64_tr_b16 a[70:71], v4 offset:7680\n"
"v_mfma_f32_16x16x32_bf16 v[76:79], a[44:47], v[56:59], 0\n"
"s_and_b32 s48, vcc_lo, vcc_hi\n"
"ds_read_b64_tr_b16 a[72:73], v5 offset:8192\n"
"ds_read_b64_tr_b16 a[74:75], v5 offset:8704\n"
"v_exp_f32 v43, v43\n"
"v_mfma_f32_16x16x32_bf16 v[112:115], a[44:47], v[60:63], 0\n"
"s_andn2_b32 s50, s47, s48\n"
"ds_read_b128 a[0:3], v6 offset:0\n"
"ds_read_b128 a[4:7], v6 offset:2048\n"
"v_mfma_f32_16x16x32_bf16 v[80:83], a[48:51], v[56:59], 0\n"
"s_cbranch_scc1 L_slowf0_%=\n"
"L_bkf0_%=:\n"
"ds_read_b128 a[8:11], v6 offset:4096\n"
"ds_read_b128 a[12:15], v6 offset:6144\n"
"v_mfma_f32_16x16x32_bf16 v[116:119], a[48:51], v[60:63], 0\n"
"v_exp_f32 v44, v44\n"
"v_mfma_f32_16x16x32_bf16 v[84:87], a[52:55], v[56:59], 0\n"
"ds_read_b64 a[16:17], v7 offset:0\n"
"v_cvt_pk_bf16_f32 v65, v42, v43\n"
"v_mfma_f32_16x16x32_bf16 v[120:123], a[52:55], v[60:63], 0\n"
"ds_read_b128 a[20:23], v6 offset:512\n"
"v_exp_f32 v45, v45\n"
"v_mfma_f32_16x16x32_bf16 v[88:91], a[56:59], v[56:59], 0\n"
"v_exp_f32 v46, v46\n"
"v_mfma_f32_16x16x32_bf16 v[124:127], a[56:59], v[60:63], 0\n"
"s_waitcnt lgkmcnt(10)\n"
"ds_read_b128 a[24:27], v6 offset:2560\n"
"v_cvt_pk_bf16_f32 v66, v44, v45\n"
"v_mfma_f32_16x16x32_bf16 v[92:95], a[60:63], v[56:59], 0\n"
"ds_read_b128 a[28:31], v6 offset:4608\n"
"v_exp_f32 v47, v47\n"
"v_mfma_f32_16x16x32_bf16 v[128:131], a[60:63], v[60:63], 0\n"
"v_exp_f32 v48, v48\n"
"v_mfma_f32_16x16x32_bf16 v[96:99], a[64:67], v[56:59], 0\n"
"ds_read_b128 a[32:35], v6 offset:6656\n"
"v_cvt_pk_bf16_f32 v67, v46, v47\n"
"v_mfma_f32_16x16x32_bf16 v[132:135], a[64:67], v[60:63], 0\n"
"s_waitcnt lgkmcnt(5)\n"
"ds_read_b64 a[36:37], v7 offset:512\n"
"v_mfma_f32_16x16x32_bf16 v[100:103], a[68:71], v[56:59], 0\n"
"v_exp_f32 v49, v49\n"
"v_mfma_f32_16x16x32_bf16 v[136:139], a[68:71], v[60:63], 0\n"
"v_mfma_f32_16x16x32_bf16 v[104:107], a[72:75], v[56:59], 0\n"
"v_mfma_f32_16x16x32_bf16 v[140:143], a[72:75], v[60:63], 0\n"
"s_waitcnt lgkmcnt(0)\n"
"L_loop_%=:\n"
"s_cmp_eq_u32 s46, s35\n"
"s_cbranch_scc1 L_wf_%=\n"
"L_wfr_%=:\n"
"v_mfma_f32_16x16x32_bf16 v[24:27], a[0:3], a[76:79], 0\n"
"v_exp_f32 v50, v50\n"
"v_mfma_f32_16x16x32_bf16 v[24:27], a[4:7], a[80:83], v[24:27]\n"
"s_add_u32 m0, s45, 0\n"
"v_cvt_pk_bf16_f32 v68, v48, v49\n"
"v_mfma_f32_16x16x32_bf16 v[24:27], a[8:11], a[84:87], v[24:27]\n"
"buffer_load_dwordx4 v10, s[40:43], s44 offen offset:0 sc1 lds\n"
"v_mfma_f32_16x16x32_bf16 v[24:27], a[12:15], a[88:91], v[24:27]\n"
"s_waitcnt lgkmcnt(3)\n"
"v_exp_f32 v51, v51\n"
"v_mfma_f32_16x16x32_bf16 v[24:27], a[16:19], a[92:95], v[24:27]\n"
"v_exp_f32 v52, v52\n"
"v_mfma_f32_16x16x32_bf16 v[28:31], a[20:23], a[76:79], 0\n"
"v_mfma_f32_16x16x32_bf16 v[28:31], a[24:27], a[80:83], v[28:31]\n"
"s_waitcnt lgkmcnt(0)\n"
"v_cvt_pk_bf16_f32 v69, v50, v51\n"
"v_mfma_f32_16x16x32_bf16 v[28:31], a[28:31], a[84:87], v[28:31]\n"
"buffer_load_dwordx4 v11, s[40:43], s44 offen offset:1024 sc1 lds\n"
"v_mfma_f32_16x16x32_bf16 v[28:31], a[32:35], a[88:91], v[28:31]\n"
"v_exp_f32 v24, v24\n"
"v_mfma_f32_16x16x32_bf16 v[28:31], a[36:39], a[92:95], v[28:31]\n"
"v_exp_f32 v53, v53\n"
"v_mfma_f32_16x16x32_bf16 v[32:35], a[0:3], a[96:99], 0\n"
"v_exp_f32 v25, v25\n"
"v_mfma_f32_16x16x32_bf16 v[32:35], a[4:7], a[100:103], v[32:35]\n"
"v_exp_f32 v54, v54\n"
"v_mfma_f32_16x16x32_bf16 v[32:35], a[8:11], a[104:107], v[32:35]\n"
"v_exp_f32 v26, v26\n"
"v_mfma_f32_16x16x32_bf16 v[32:35], a[12:15], a[108:111], v[32:35]\n"
"buffer_load_dwordx4 v12, s[40:43], s44 offen offset:2048 sc1 lds\n"
"v_mfma_f32_16x16x32_bf16 v[32:35], a[16:19], a[112:115], v[32:35]\n"
"v_cvt_pk_bf16_f32 v70, v52, v53\n"
"v_cvt_pk_bf16_f32 v56, v24, v25\n"
"v_mfma_f32_16x16x32_bf16 v[36:39], a[20:23], a[96:99], 0\n"
"v_exp_f32 v55, v55\n"
"v_mfma_f32_16x16x32_bf16 v[36:39], a[24:27], a[100:103], v[36:39]\n"
"v_exp_f32 v27, v27\n"
"v_mfma_f32_16x16x32_bf16 v[36:39], a[28:31], a[104:107], v[36:39]\n"
"v_cvt_pk_bf16_f32 v71, v54, v55\n"
"v_mfma_f32_16x16x32_bf16 v[36:39], a[32:35], a[108:111], v[36:39]\n"
"v_exp_f32 v28, v28\n"
"v_mfma_f32_16x16x32_bf16 v[36:39], a[36:39], a[112:115], v[36:39]\n"
"buffer_load_dwordx4 v13, s[40:43], s44 offen offset:3072 sc1 lds\n"
"v_mfma_f32_16x16x32_bf16 v[144:147], a[40:43], v[64:67], v[144:147]\n"
"v_cvt_pk_bf16_f32 v57, v26, v27\n"
"v_mfma_f32_16x16x32_bf16 v[180:183], a[40:43], v[68:71], v[180:183]\n"
"ds_read_b64_tr_b16 a[40:41], v8 offset:0\n"
"v_mfma_f32_16x16x32_bf16 v[148:151], a[44:47], v[64:67], v[148:151]\n"
"ds_read_b64_tr_b16 a[42:43], v8 offset:512\n"
"v_mfma_f32_16x16x32_bf16 v[184:187], a[44:47], v[68:71], v[184:187]\n"
"v_exp_f32 v29, v29\n"
"v_mfma_f32_16x16x32_bf16 v[152:155], a[48:51], v[64:67], v[152:155]\n"
"s_add_u32 m0, s45, 4096\n"
"v_exp_f32 v30, v30\n"
"v_mfma_f32_16x16x32_bf16 v[188:191], a[48:51], v[68:71], v[188:191]\n"
"buffer_load_dwordx4 v14, s[40:43], s44 offen offset:0 sc1 lds\n"
"v_mfma_f32_16x16x32_bf16 v[156:159], a[52:55], v[64:67], v[156:159]\n"
"ds_read_b64_tr_b16 a[44:45], v8 offset:1024\n"
"v_cvt_pk_bf16_f32 v58, v28, v29\n"
"v_mfma_f32_16x16x32_bf16 v[192:195], a[52:55], v[68:71], v[192:195]\n"
"ds_read_b64_tr_b16 a[46:47], v8 offset:1536\n"
"v_mfma_f32_16x16x32_bf16 v[160:163], a[56:59], v[64:67], v[160:163]\n"
"v_exp_f32 v31, v31\n"
"v_mfma_f32_16x16x32_bf16 v[196:199], a[56:59], v[68:71], v[196:199]\n"
"ds_read_b64_tr_b16 a[48:49], v8 offset:2048\n"
"v_mfma_f32_16x16x32_bf16 v[164:167], a[60:63], v[64:67], v[164:167]\n"
"buffer_load_dwordx4 v15, s[40:43], s44 offen offset:1024 sc1 lds\n"
"v_mfma_f32_16x16x32_bf16 v[200:203], a[60:63], v[68:71], v[200:203]\n"
"v_exp_f32 v32, v32\n"
"v_mfma_f32_16x16x32_bf16 v[168:171], a[64:67], v[64:67], v[168:171]\n"
"ds_read_b64_tr_b16 a[50:51], v8 offset:2560\n"
"v_cvt_pk_bf16_f32 v59, v30, v31\n"
"v_mfma_f32_16x16x32_bf16 v[204:207], a[64:67], v[68:71], v[204:207]\n"
"ds_read_b64_tr_b16 a[52:53], v8 offset:3072\n"
"ds_read_b64_tr_b16 a[54:55], v8 offset:3584\n"
"v_mfma_f32_16x16x32_bf16 v[172:175], a[68:71], v[64:67], v[172:175]\n"
"v_exp_f32 v33, v33\n"
"v_mfma_f32_16x16x32_bf16 v[208:211], a[68:71], v[68:71], v[208:211]\n"
"ds_read_b64_tr_b16 a[56:57], v8 offset:4096\n"
"v_mfma_f32_16x16x32_bf16 v[176:179], a[72:75], v[64:67], v[176:179]\n"
"buffer_load_dwordx4 v16, s[40:43], s44 offen offset:2048 sc1 lds\n"
"v_mfma_f32_16x16x32_bf16 v[212:215], a[72:75], v[68:71], v[212:215]\n"
"v_exp_f32 v34, v34\n"
"v_mfma_f32_16x16x32_bf16 v[40:43], a[0:3], a[116:119], 0\n"
"ds_read_b64_tr_b16 a[58:59], v8 offset:4608\n"
"v_cvt_pk_bf16_f32 v60, v32, v33\n"
"v_mfma_f32_16x16x32_bf16 v[40:43], a[4:7], a[120:123], v[40:43]\n"
"ds_read_b64_tr_b16 a[60:61], v8 offset:5120\n"
"v_mfma_f32_16x16x32_bf16 v[40:43], a[8:11], a[124:127], v[40:43]\n"
"v_exp_f32 v35, v35\n"
"v_mfma_f32_16x16x32_bf16 v[40:43], a[12:15], a[128:131], v[40:43]\n"
"ds_read_b64_tr_b16 a[62:63], v8 offset:5632\n"
"v_mfma_f32_16x16x32_bf16 v[40:43], a[16:19], a[132:135], v[40:43]\n"
"buffer_load_dwordx4 v17, s[40:43], s44 offen offset:3072 sc1 lds\n"
"v_mfma_f32_16x16x32_bf16 v[44:47], a[20:23], a[116:119], 0\n"
"v_exp_f32 v36, v36\n"
"v_mfma_f32_16x16x32_bf16 v[44:47], a[24:27], a[120:123], v[44:47]\n"
"ds_read_b64_tr_b16 a[64:65], v8 offset:6144\n"
"v_cvt_pk_bf16_f32 v61, v34, v35\n"
"v_mfma_f32_16x16x32_bf16 v[44:47], a[28:31], a[124:127], v[44:47]\n"
"ds_read_b64_tr_b16 a[66:67], v8 offset:6656\n"
"v_mfma_f32_16x16x32_bf16 v[44:47], a[32:35], a[128:131], v[44:47]\n"
"v_exp_f32 v37, v37\n"
"v_mfma_f32_16x16x32_bf16 v[44:47], a[36:39], a[132:135], v[44:47]\n"
"s_add_u32 m0, s45, 8192\n"
"ds_read_b64_tr_b16 a[68:69], v8 offset:7168\n"
"v_mfma_f32_16x16x32_bf16 v[48:51], a[0:3], a[136:139], 0\n"
"v_exp_f32 v38, v38\n"
"v_mfma_f32_16x16x32_bf16 v[48:51], a[4:7], a[140:143], v[48:51]\n"
"buffer_load_dwordx4 v18, s[40:43], s44 offen offset:0 sc1 lds\n"
"v_mfma_f32_16x16x32_bf16 v[48:51], a[8:11], a[144:147], v[48:51]\n"
"s_add_u32 s44, s44, 9216\n"
"ds_read_b64_tr_b16 a[70:71], v8 offset:7680\n"
"v_cvt_pk_bf16_f32 v62, v36, v37\n"
"v_mfma_f32_16x16x32_bf16 v[48:51], a[12:15], a[148:151], v[48:51]\n"
"ds_read_b64_tr_b16 a[72:73], v9 offset:8192\n"
"v_mfma_f32_16x16x32_bf16 v[48:51], a[16:19], a[152:155], v[48:51]\n"
"v_exp_f32 v39, v39\n"
"v_mfma_f32_16x16x32_bf16 v[52:55], a[20:23], a[136:139], 0\n"
"ds_read_b64_tr_b16 a[74:75], v9 offset:8704\n"
"v_mfma_f32_16x16x32_bf16 v[52:55], a[24:27], a[140:143], v[52:55]\n"
"v_cvt_pk_bf16_f32 v63, v38, v39\n"
"v_mfma_f32_16x16x32_bf16 v[52:55], a[28:31], a[144:147], v[52:55]\n"
"v_exp_f32 v40, v40\n"
"v_mfma_f32_16x16x32_bf16 v[52:55], a[32:35], a[148:151], v[52:55]\n"
"v_exp_f32 v41, v41\n"
"v_mfma_f32_16x16x32_bf16 v[52:55], a[36:39], a[152:155], v[52:55]\n"
"s_waitcnt vmcnt(9) lgkmcnt(3)\n"
"v_exp_f32 v42, v42\n"
"v_mfma_f32_16x16x32_bf16 v[72:75], a[40:43], v[56:59], v[72:75]\n"
"ds_read_b128 a[0:3], v2 offset:36864\n"
"v_cvt_pk_bf16_f32 v64, v40, v41\n"
"v_mfma_f32_16x16x32_bf16 v[108:111], a[40:43], v[60:63], v[108:111]\n"
"ds_read_b128 a[4:7], v2 offset:38912\n"
"v_mfma_f32_16x16x32_bf16 v[76:79], a[44:47], v[56:59], v[76:79]\n"
"v_exp_f32 v43, v43\n"
"v_mfma_f32_16x16x32_bf16 v[112:115], a[44:47], v[60:63], v[112:115]\n"
"ds_read_b128 a[8:11], v2 offset:40960\n"
"v_mfma_f32_16x16x32_bf16 v[80:83], a[48:51], v[56:59], v[80:83]\n"
"ds_read_b128 a[12:15], v2 offset:43008\n"
"v_mfma_f32_16x16x32_bf16 v[116:119], a[48:51], v[60:63], v[116:119]\n"
"v_exp_f32 v44, v44\n"
"v_mfma_f32_16x16x32_bf16 v[84:87], a[52:55], v[56:59], v[84:87]\n"
"ds_read_b64 a[16:17], v3 offset:36864\n"
"v_cvt_pk_bf16_f32 v65, v42, v43\n"
"v_mfma_f32_16x16x32_bf16 v[120:123], a[52:55], v[60:63], v[120:123]\n"
"v_exp_f32 v45, v45\n"
"v_mfma_f32_16x16x32_bf16 v[88:91], a[56:59], v[56:59], v[88:91]\n"
"v_exp_f32 v46, v46\n"
"v_mfma_f32_16x16x32_bf16 v[124:127], a[56:59], v[60:63], v[124:127]\n"
"ds_read_b128 a[20:23], v2 offset:37376\n"
"v_cvt_pk_bf16_f32 v66, v44, v45\n"
"v_mfma_f32_16x16x32_bf16 v[92:95], a[60:63], v[56:59], v[92:95]\n"
"v_exp_f32 v47, v47\n"
"v_mfma_f32_16x16x32_bf16 v[128:131], a[60:63], v[60:63], v[128:131]\n"
"v_exp_f32 v48, v48\n"
"v_mfma_f32_16x16x32_bf16 v[96:99], a[64:67], v[56:59], v[96:99]\n"
"ds_read_b128 a[24:27], v2 offset:39424\n"
"v_cvt_pk_bf16_f32 v67, v46, v47\n"
"v_mfma_f32_16x16x32_bf16 v[132:135], a[64:67], v[60:63], v[132:135]\n"
"s_waitcnt lgkmcnt(3)\n"
"ds_read_b128 a[28:31], v2 offset:41472\n"
"ds_read_b128 a[32:35], v2 offset:43520\n"
"v_mfma_f32_16x16x32_bf16 v[100:103], a[68:71], v[56:59], v[100:103]\n"
"v_exp_f32 v49, v49\n"
"v_mfma_f32_16x16x32_bf16 v[136:139], a[68:71], v[60:63], v[136:139]\n"
"ds_read_b64 a[36:37], v3 offset:37376\n"
"v_mfma_f32_16x16x32_bf16 v[104:107], a[72:75], v[56:59], v[104:107]\n"
"v_mfma_f32_16x16x32_bf16 v[140:143], a[72:75], v[60:63], v[140:143]\n"
"L_loop2_%=:\n"
"v_mfma_f32_16x16x32_bf16 v[24:27], a[0:3], a[76:79], 0\n"
"v_exp_f32 v50, v50\n"
"v_mfma_f32_16x16x32_bf16 v[24:27], a[4:7], a[80:83], v[24:27]\n"
"s_add_u32 m0, s45, 73728\n"
"v_cvt_pk_bf16_f32 v68, v48, v49\n"
"v_mfma_f32_16x16x32_bf16 v[24:27], a[8:11], a[84:87], v[24:27]\n"
"buffer_load_dwordx4 v10, s[40:43], s44 offen offset:0 sc1 lds\n"
"v_mfma_f32_16x16x32_bf16 v[24:27], a[12:15], a[88:91], v[24:27]\n"
"s_waitcnt lgkmcnt(3)\n"
"v_exp_f32 v51, v51\n"
"v_mfma_f32_16x16x32_bf16 v[24:27], a[16:19], a[92:95], v[24:27]\n"
"v_exp_f32 v52, v52\n"
"v_mfma_f32_16x16x32_bf16 v[28:31], a[20:23], a[76:79], 0\n"
"v_mfma_f32_16x16x32_bf16 v[28:31], a[24:27], a[80:83], v[28:31]\n"
"s_waitcnt lgkmcnt(0)\n"
"v_cvt_pk_bf16_f32 v69, v50, v51\n"
"v_mfma_f32_16x16x32_bf16 v[28:31], a[28:31], a[84:87], v[28:31]\n"
"buffer_load_dwordx4 v11, s[40:43], s44 offen offset:1024 sc1 lds\n"
"v_mfma_f32_16x16x32_bf16 v[28:31], a[32:35], a[88:91], v[28:31]\n"
"v_exp_f32 v24, v24\n"
"v_mfma_f32_16x16x32_bf16 v[28:31], a[36:39], a[92:95], v[28:31]\n"
"v_exp_f32 v53, v53\n"
"v_mfma_f32_16x16x32_bf16 v[32:35], a[0:3], a[96:99], 0\n"
"v_exp_f32 v25, v25\n"
"v_mfma_f32_16x16x32_bf16 v[32:35], a[4:7], a[100:103], v[32:35]\n"
"v_exp_f32 v54, v54\n"
"v_mfma_f32_16x16x32_bf16 v[32:35], a[8:11], a[104:107], v[32:35]\n"
"v_exp_f32 v26, v26\n"
"v_mfma_f32_16x16x32_bf16 v[32:35], a[12:15], a[108:111], v[32:35]\n"
"buffer_load_dwordx4 v12, s[40:43], s44 offen offset:2048 sc1 lds\n"
"v_mfma_f32_16x16x32_bf16 v[32:35], a[16:19], a[112:115], v[32:35]\n"
"v_cvt_pk_bf16_f32 v70, v52, v53\n"
"v_cvt_pk_bf16_f32 v56, v24, v25\n"
"v_mfma_f32_16x16x32_bf16 v[36:39], a[20:23], a[96:99], 0\n"
"v_exp_f32 v55, v55\n"
"v_mfma_f32_16x16x32_bf16 v[36:39], a[24:27], a[100:103], v[36:39]\n"
"v_exp_f32 v27, v27\n"
"v_mfma_f32_16x16x32_bf16 v[36:39], a[28:31], a[104:107], v[36:39]\n"
"v_cvt_pk_bf16_f32 v71, v54, v55\n"
"v_mfma_f32_16x16x32_bf16 v[36:39], a[32:35], a[108:111], v[36:39]\n"
"v_exp_f32 v28, v28\n"
"v_mfma_f32_16x16x32_bf16 v[36:39], a[36:39], a[112:115], v[36:39]\n"
"buffer_load_dwordx4 v13, s[40:43], s44 offen offset:3072 sc1 lds\n"
"v_mfma_f32_16x16x32_bf16 v[144:147], a[40:43], v[64:67], v[144:147]\n"
"v_cvt_pk_bf16_f32 v57, v26, v27\n"
"v_mfma_f32_16x16x32_bf16 v[180:183], a[40:43], v[68:71], v[180:183]\n"
"ds_read_b64_tr_b16 a[40:41], v4 offset:36864\n"
"v_mfma_f32_16x16x32_bf16 v[148:151], a[44:47], v[64:67], v[148:151]\n"
"ds_read_b64_tr_b16 a[42:43], v4 offset:37376\n"
"v_mfma_f32_16x16x32_bf16 v[184:187], a[44:47], v[68:71], v[184:187]\n"
"v_exp_f32 v29, v29\n"
"v_mfma_f32_16x16x32_bf16 v[152:155], a[48:51], v[64:67], v[152:155]\n"
"s_add_u32 m0, s45, 77824\n"
"v_exp_f32 v30, v30\n"
"v_mfma_f32_16x16x32_bf16 v[188:191], a[48:51], v[68:71], v[188:191]\n"
"buffer_load_dwordx4 v14, s[40:43], s44 offen offset:0 sc1 lds\n"
"v_mfma_f32_16x16x32_bf16 v[156:159], a[52:55], v[64:67], v[156:159]\n"
"ds_read_b64_tr_b16 a[44:45], v4 offset:37888\n"
"v_cvt_pk_bf16_f32 v58, v28, v29\n"
"v_mfma_f32_16x16x32_bf16 v[192:195], a[52:55], v[68:71], v[192:195]\n"
"ds_read_b64_tr_b16 a[46:47], v4 offset:38400\n"
"v_mfma_f32_16x16x32_bf16 v[160:163], a[56:59], v[64:67], v[160:163]\n"
"v_exp_f32 v31, v31\n"
"v_mfma_f32_16x16x32_bf16 v[196:199], a[56:59], v[68:71], v[196:199]\n"
"ds_read_b64_tr_b16 a[48:49], v4 offset:38912\n"
"v_mfma_f32_16x16x32_bf16 v[164:167], a[60:63], v[64:67], v[164:167]\n"
"buffer_load_dwordx4 v15, s[40:43], s44 offen offset:1024 sc1 lds\n"
"v_mfma_f32_16x16x32_bf16 v[200:203], a[60:63], v[68:71], v[200:203]\n"
"v_exp_f32 v32, v32\n"
"v_mfma_f32_16x16x32_bf16 v[168:171], a[64:67], v[64:67], v[168:171]\n"
"ds_read_b64_tr_b16 a[50:51], v4 offset:39424\n"
"v_cvt_pk_bf16_f32 v59, v30, v31\n"
"v_mfma_f32_16x16x32_bf16 v[204:207], a[64:67], v[68:71], v[204:207]\n"
"ds_read_b64_tr_b16 a[52:53], v4 offset:39936\n"
"ds_read_b64_tr_b16 a[54:55], v4 offset:40448\n"
"v_mfma_f32_16x16x32_bf16 v[172:175], a[68:71], v[64:67], v[172:175]\n"
"v_exp_f32 v33, v33\n"
"v_mfma_f32_16x16x32_bf16 v[208:211], a[68:71], v[68:71], v[208:211]\n"
"ds_read_b64_tr_b16 a[56:57], v4 offset:40960\n"
"v_mfma_f32_16x16x32_bf16 v[176:179], a[72:75], v[64:67], v[176:179]\n"
"buffer_load_dwordx4 v16, s[40:43], s44 offen offset:2048 sc1 lds\n"
"v_mfma_f32_16x16x32_bf16 v[212:215], a[72:75], v[68:71], v[212:215]\n"
"v_exp_f32 v34, v34\n"
"v_mfma_f32_16x16x32_bf16 v[40:43], a[0:3], a[116:119], 0\n"
"ds_read_b64_tr_b16 a[58:59], v4 offset:41472\n"
"v_cvt_pk_bf16_f32 v60, v32, v33\n"
"v_mfma_f32_16x16x32_bf16 v[40:43], a[4:7], a[120:123], v[40:43]\n"
"ds_read_b64_tr_b16 a[60:61], v4 offset:41984\n"
"v_mfma_f32_16x16x32_bf16 v[40:43], a[8:11], a[124:127], v[40:43]\n"
"v_exp_f32 v35, v35\n"
"v_mfma_f32_16x16x32_bf16 v[40:43], a[12:15], a[128:131], v[40:43]\n"
"ds_read_b64_tr_b16 a[62:63], v4 offset:42496\n"
"v_mfma_f32_16x16x32_bf16 v[40:43], a[16:19], a[132:135], v[40:43]\n"
"buffer_load_dwordx4 v17, s[40:43], s44 offen offset:3072 sc1 lds\n"
"v_mfma_f32_16x16x32_bf16 v[44:47], a[20:23], a[116:119], 0\n"
"v_exp_f32 v36, v36\n"
"v_mfma_f32_16x16x32_bf16 v[44:47], a[24:27], a[120:123], v[44:47]\n"
"ds_read_b64_tr_b16 a[64:65], v4 offset:43008\n"
"v_cvt_pk_bf16_f32 v61, v34, v35\n"
"v_mfma_f32_16x16x32_bf16 v[44:47], a[28:31], a[124:127], v[44:47]\n"
"ds_read_b64_tr_b16 a[66:67], v4 offset:43520\n"
"v_mfma_f32_16x16x32_bf16 v[44:47], a[32:35], a[128:131], v[44:47]\n"
"v_exp_f32 v37, v37\n"
"v_mfma_f32_16x16x32_bf16 v[44:47], a[36:39], a[132:135], v[44:47]\n"
"s_add_u32 m0, s45, 81920\n"
"ds_read_b64_tr_b16 a[68:69], v4 offset:44032\n"
"v_mfma_f32_16x16x32_bf16 v[48:51], a[0:3], a[136:139], 0\n"
"v_exp_f32 v38, v38\n"
"v_mfma_f32_16x16x32_bf16 v[48:51], a[4:7], a[140:143], v[48:51]\n"
"buffer_load_dwordx4 v18, s[40:43], s44 offen offset:0 sc1 lds\n"
"v_mfma_f32_16x16x32_bf16 v[48:51], a[8:11], a[144:147], v[48:51]\n"
"s_add_u32 s44, s44, 9216\n"
"ds_read_b64_tr_b16 a[70:71], v4 offset:44544\n"
"v_cvt_pk_bf16_f32 v62, v36, v37\n"
"v_mfma_f32_16x16x32_bf16 v[48:51], a[12:15], a[148:151], v[48:51]\n"
"ds_read_b64_tr_b16 a[72:73], v5 offset:45056\n"
"v_mfma_f32_16x16x32_bf16 v[48:51], a[16:19], a[152:155], v[48:51]\n"
"v_exp_f32 v39, v39\n"
"v_mfma_f32_16x16x32_bf16 v[52:55], a[20:23], a[136:139], 0\n"
"ds_read_b64_tr_b16 a[74:75], v5 offset:45568\n"
"v_mfma_f32_16x16x32_bf16 v[52:55], a[24:27], a[140:143], v[52:55]\n"
"v_cvt_pk_bf16_f32 v63, v38, v39\n"
"v_mfma_f32_16x16x32_bf16 v[52:55], a[28:31], a[144:147], v[52:55]\n"
"v_exp_f32 v40, v40\n"
"v_mfma_f32_16x16x32_bf16 v[52:55], a[32:35], a[148:151], v[52:55]\n"
"v_exp_f32 v41, v41\n"
"v_mfma_f32_16x16x32_bf16 v[52:55], a[36:39], a[152:155], v[52:55]\n"
"s_waitcnt vmcnt(9) lgkmcnt(3)\n"
"v_exp_f32 v42, v42\n"
"v_mfma_f32_16x16x32_bf16 v[72:75], a[40:43], v[56:59], v[72:75]\n"
"ds_read_b128 a[0:3], v2 offset:0\n"
"v_cvt_pk_bf16_f32 v64, v40, v41\n"
"v_mfma_f32_16x16x32_bf16 v[108:111], a[40:43], v[60:63], v[108:111]\n"
"ds_read_b128 a[4:7], v2 offset:2048\n"
"v_mfma_f32_16x16x32_bf16 v[76:79], a[44:47], v[56:59], v[76:79]\n"
"v_exp_f32 v43, v43\n"
"v_mfma_f32_16x16x32_bf16 v[112:115], a[44:47], v[60:63], v[112:115]\n"
"ds_read_b128 a[8:11], v2 offset:4096\n"
"v_mfma_f32_16x16x32_bf16 v[80:83], a[48:51], v[56:59], v[80:83]\n"
"ds_read_b128 a[12:15], v2 offset:6144\n"
"v_mfma_f32_16x16x32_bf16 v[116:119], a[48:51], v[60:63], v[116:119]\n"
"v_exp_f32 v44, v44\n"
"v_mfma_f32_16x16x32_bf16 v[84:87], a[52:55], v[56:59], v[84:87]\n"
"ds_read_b64 a[16:17], v3 offset:0\n"
"v_cvt_pk_bf16_f32 v65, v42, v43\n"
"v_mfma_f32_16x16x32_bf16 v[120:123], a[52:55], v[60:63], v[120:123]\n"
"v_exp_f32 v45, v45\n"
"v_mfma_f32_16x16x32_bf16 v[88:91], a[56:59], v[56:59], v[88:91]\n"
"v_exp_f32 v46, v46\n"
"v_mfma_f32_16x16x32_bf16 v[124:127], a[56:59], v[60:63], v[124:127]\n"
"ds_read_b128 a[20:23], v2 offset:512\n"
"v_cvt_pk_bf16_f32 v66, v44, v45\n"
"v_mfma_f32_16x16x32_bf16 v[92:95], a[60:63], v[56:59], v[92:95]\n"
"v_exp_f32 v47, v47\n"
"v_mfma_f32_16x16x32_bf16 v[128:131], a[60:63], v[60:63], v[128:131]\n"
"v_exp_f32 v48, v48\n"
"v_mfma_f32_16x16x32_bf16 v[96:99], a[64:67], v[56:59], v[96:99]\n"
"ds_read_b128 a[24:27], v2 offset:2560\n"
"v_cvt_pk_bf16_f32 v67, v46, v47\n"
"v_mfma_f32_16x16x32_bf16 v[132:135], a[64:67], v[60:63], v[132:135]\n"
"s_waitcnt lgkmcnt(3)\n"
"ds_read_b128 a[28:31], v2 offset:4608\n"
"ds_read_b128 a[32:35], v2 offset:6656\n"
"v_mfma_f32_16x16x32_bf16 v[100:103], a[68:71], v[56:59], v[100:103]\n"
"v_exp_f32 v49, v49\n"
"v_mfma_f32_16x16x32_bf16 v[136:139], a[68:71], v[60:63], v[136:139]\n"
"ds_read_b64 a[36:37], v3 offset:512\n"
"v_mfma_f32_16x16x32_bf16 v[104:107], a[72:75], v[56:59], v[104:107]\n"
"v_mfma_f32_16x16x32_bf16 v[140:143], a[72:75], v[60:63], v[140:143]\n"
"v_mfma_f32_16x16x32_bf16 v[24:27], a[0:3], a[76:79], 0\n"
"v_exp_f32 v50, v50\n"
"v_mfma_f32_16x16x32_bf16 v[24:27], a[4:7], a[80:83], v[24:27]\n"
"s_add_u32 m0, s45, 36864\n"
"v_cvt_pk_bf16_f32 v68, v48, v49\n"
"v_mfma_f32_16x16x32_bf16 v[24:27], a[8:11], a[84:87], v[24:27]\n"
"buffer_load_dwordx4 v10, s[40:43], s44 offen offset:0 sc1 lds\n"
"v_mfma_f32_16x16x32_bf16 v[24:27], a[12:15], a[88:91], v[24:27]\n"
"s_waitcnt lgkmcnt(3)\n"
"v_exp_f32 v51, v51\n"
"v_mfma_f32_16x16x32_bf16 v[24:27], a[16:19], a[92:95], v[24:27]\n"
"v_exp_f32 v52, v52\n"
"v_mfma_f32_16x16x32_bf16 v[28:31], a[20:23], a[76:79], 0\n"
"v_mfma_f32_16x16x32_bf16 v[28:31], a[24:27], a[80:83], v[28:31]\n"
"s_waitcnt lgkmcnt(0)\n"
"v_cvt_pk_bf16_f32 v69, v50, v51\n"
"v_mfma_f32_16x16x32_bf16 v[28:31], a[28:31], a[84:87], v[28:31]\n"
"buffer_load_dwordx4 v11, s[40:43], s44 offen offset:1024 sc1 lds\n"
"v_mfma_f32_16x16x32_bf16 v[28:31], a[32:35], a[88:91], v[28:31]\n"
"v_exp_f32 v24, v24\n"
"v_mfma_f32_16x16x32_bf16 v[28:31], a[36:39], a[92:95], v[28:31]\n"
"v_exp_f32 v53, v53\n"
"v_mfma_f32_16x16x32_bf16 v[32:35], a[0:3], a[96:99], 0\n"
"v_exp_f32 v25, v25\n"
"v_mfma_f32_16x16x32_bf16 v[32:35], a[4:7], a[100:103], v[32:35]\n"
"v_exp_f32 v54, v54\n"
"v_mfma_f32_16x16x32_bf16 v[32:35], a[8:11], a[104:107], v[32:35]\n"
"v_exp_f32 v26, v26\n"
"v_mfma_f32_16x16x32_bf16 v[32:35], a[12:15], a[108:111], v[32:35]\n"
"buffer_load_dwordx4 v12, s[40:43], s44 offen offset:2048 sc1 lds\n"
"v_mfma_f32_16x16x32_bf16 v[32:35], a[16:19], a[112:115], v[32:35]\n"
"v_cvt_pk_bf16_f32 v70, v52, v53\n"
"v_cvt_pk_bf16_f32 v56, v24, v25\n"
"v_mfma_f32_16x16x32_bf16 v[36:39], a[20:23], a[96:99], 0\n"
"v_exp_f32 v55, v55\n"
"v_mfma_f32_16x16x32_bf16 v[36:39], a[24:27], a[100:103], v[36:39]\n"
"v_exp_f32 v27, v27\n"
"v_mfma_f32_16x16x32_bf16 v[36:39], a[28:31], a[104:107], v[36:39]\n"
"v_cvt_pk_bf16_f32 v71, v54, v55\n"
"v_mfma_f32_16x16x32_bf16 v[36:39], a[32:35], a[108:111], v[36:39]\n"
"v_exp_f32 v28, v28\n"
"v_mfma_f32_16x16x32_bf16 v[36:39], a[36:39], a[112:115], v[36:39]\n"
"buffer_load_dwordx4 v13, s[40:43], s44 offen offset:3072 sc1 lds\n"
"v_mfma_f32_16x16x32_bf16 v[144:147], a[40:43], v[64:67], v[144:147]\n"
"v_cvt_pk_bf16_f32 v57, v26, v27\n"
"v_mfma_f32_16x16x32_bf16 v[180:183], a[40:43], v[68:71], v[180:183]\n"
"ds_read_b64_tr_b16 a[40:41], v4 offset:0\n"
"v_mfma_f32_16x16x32_bf16 v[148:151], a[44:47], v[64:67], v[148:151]\n"
"ds_read_b64_tr_b16 a[42:43], v4 offset:512\n"
"v_mfma_f32_16x16x32_bf16 v[184:187], a[44:47], v[68:71], v[184:187]\n"
"v_exp_f32 v29, v29\n"
"v_mfma_f32_16x16x32_bf16 v[152:155], a[48:51], v[64:67], v[152:155]\n"
"s_add_u32 m0, s45, 40960\n"
"v_exp_f32 v30, v30\n"
"v_mfma_f32_16x16x32_bf16 v[188:191], a[48:51], v[68:71], v[188:191]\n"
"buffer_load_dwordx4 v14, s[40:43], s44 offen offset:0 sc1 lds\n"
"v_mfma_f32_16x16x32_bf16 v[156:159], a[52:55], v[64:67], v[156:159]\n"
"ds_read_b64_tr_b16 a[44:45], v4 offset:1024\n"
"v_cvt_pk_bf16_f32 v58, v28, v29\n"
"v_mfma_f32_16x16x32_bf16 v[192:195], a[52:55], v[68:71], v[192:195]\n"
"ds_read_b64_tr_b16 a[46:47], v4 offset:1536\n"
"v_mfma_f32_16x16x32_bf16 v[160:163], a[56:59], v[64:67], v[160:163]\n"
"v_exp_f32 v31, v31\n"
"v_mfma_f32_16x16x32_bf16 v[196:199], a[56:59], v[68:71], v[196:199]\n"
"ds_read_b64_tr_b16 a[48:49], v4 offset:2048\n"
"v_mfma_f32_16x16x32_bf16 v[164:167], a[60:63], v[64:67], v[164:167]\n"
"buffer_load_dwordx4 v15, s[40:43], s44 offen offset:1024 sc1 lds\n"
"v_mfma_f32_16x16x32_bf16 v[200:203], a[60:63], v[68:71], v[200:203]\n"
"v_exp_f32 v32, v32\n"
"v_mfma_f32_16x16x32_bf16 v[168:171], a[64:67], v[64:67], v[168:171]\n"
"ds_read_b64_tr_b16 a[50:51], v4 offset:2560\n"
"v_cvt_pk_bf16_f32 v59, v30, v31\n"
"v_mfma_f32_16x16x32_bf16 v[204:207], a[64:67], v[68:71], v[204:207]\n"
"ds_read_b64_tr_b16 a[52:53], v4 offset:3072\n"
"ds_read_b64_tr_b16 a[54:55], v4 offset:3584\n"
"v_mfma_f32_16x16x32_bf16 v[172:175], a[68:71], v[64:67], v[172:175]\n"
"v_exp_f32 v33, v33\n"
"v_mfma_f32_16x16x32_bf16 v[208:211], a[68:71], v[68:71], v[208:211]\n"
"ds_read_b64_tr_b16 a[56:57], v4 offset:4096\n"
"v_mfma_f32_16x16x32_bf16 v[176:179], a[72:75], v[64:67], v[176:179]\n"
"buffer_load_dwordx4 v16, s[40:43], s44 offen offset:2048 sc1 lds\n"
"v_mfma_f32_16x16x32_bf16 v[212:215], a[72:75], v[68:71], v[212:215]\n"
"v_exp_f32 v34, v34\n"
"v_mfma_f32_16x16x32_bf16 v[40:43], a[0:3], a[116:119], 0\n"
"ds_read_b64_tr_b16 a[58:59], v4 offset:4608\n"
"v_cvt_pk_bf16_f32 v60, v32, v33\n"
"v_mfma_f32_16x16x32_bf16 v[40:43], a[4:7], a[120:123], v[40:43]\n"
"ds_read_b64_tr_b16 a[60:61], v4 offset:5120\n"
"v_mfma_f32_16x16x32_bf16 v[40:43], a[8:11], a[124:127], v[40:43]\n"
"v_exp_f32 v35, v35\n"
"v_mfma_f32_16x16x32_bf16 v[40:43], a[12:15], a[128:131], v[40:43]\n"
"ds_read_b64_tr_b16 a[62:63], v4 offset:5632\n"
"v_mfma_f32_16x16x32_bf16 v[40:43], a[16:19], a[132:135], v[40:43]\n"
"buffer_load_dwordx4 v17, s[40:43], s44 offen offset:3072 sc1 lds\n"
"v_mfma_f32_16x16x32_bf16 v[44:47], a[20:23], a[116:119], 0\n"
"v_exp_f32 v36, v36\n"
"v_mfma_f32_16x16x32_bf16 v[44:47], a[24:27], a[120:123], v[44:47]\n"
"ds_read_b64_tr_b16 a[64:65], v4 offset:6144\n"
"v_cvt_pk_bf16_f32 v61, v34, v35\n"
"v_mfma_f32_16x16x32_bf16 v[44:47], a[28:31], a[124:127], v[44:47]\n"
"ds_read_b64_tr_b16 a[66:67], v4 offset:6656\n"
"v_mfma_f32_16x16x32_bf16 v[44:47], a[32:35], a[128:131], v[44:47]\n"
"v_exp_f32 v37, v37\n"
"v_mfma_f32_16x16x32_bf16 v[44:47], a[36:39], a[132:135], v[44:47]\n"
"s_add_u32 m0, s45, 45056\n"
"ds_read_b64_tr_b16 a[68:69], v4 offset:7168\n"
"v_mfma_f32_16x16x32_bf16 v[48:51], a[0:3], a[136:139], 0\n"
"v_exp_f32 v38, v38\n"
"v_mfma_f32_16x16x32_bf16 v[48:51], a[4:7], a[140:143], v[48:51]\n"
"buffer_load_dwordx4 v18, s[40:43], s44 offen offset:0 sc1 lds\n"
"v_mfma_f32_16x16x32_bf16 v[48:51], a[8:11], a[144:147], v[48:51]\n"
"s_add_u32 s44, s44, 9216\n"
"ds_read_b64_tr_b16 a[70:71], v4 offset:7680\n"
"v_cvt_pk_bf16_f32 v62, v36, v37\n"
"v_mfma_f32_16x16x32_bf16 v[48:51], a[12:15], a[148:151], v[48:51]\n"
"ds_read_b64_tr_b16 a[72:73], v5 offset:8192\n"
"v_mfma_f32_16x16x32_bf16 v[48:51], a[16:19], a[152:155], v[48:51]\n"
"v_exp_f32 v39, v39\n"
"v_mfma_f32_16x16x32_bf16 v[52:55], a[20:23], a[136:139], 0\n"
"ds_read_b64_tr_b16 a[74:75], v5 offset:8704\n"
"v_mfma_f32_16x16x32_bf16 v[52:55], a[24:27], a[140:143], v[52:55]\n"
"v_cvt_pk_bf16_f32 v63, v38, v39\n"
"v_mfma_f32_16x16x32_bf16 v[52:55], a[28:31], a[144:147], v[52:55]\n"
"v_exp_f32 v40, v40\n"
"v_mfma_f32_16x16x32_bf16 v[52:55], a[32:35], a[148:151], v[52:55]\n"
"v_exp_f32 v41, v41\n"
"v_mfma_f32_16x16x32_bf16 v[52:55], a[36:39], a[152:155], v[52:55]\n"
"s_waitcnt vmcnt(9) lgkmcnt(3)\n"
"v_exp_f32 v42, v42\n"
"v_mfma_f32_16x16x32_bf16 v[72:75], a[40:43], v[56:59], v[72:75]\n"
"ds_read_b128 a[0:3], v6 offset:0\n"
"v_cvt_pk_bf16_f32 v64, v40, v41\n"
"v_mfma_f32_16x16x32_bf16 v[108:111], a[40:43], v[60:63], v[108:111]\n"
"ds_read_b128 a[4:7], v6 offset:2048\n"
"v_mfma_f32_16x16x32_bf16 v[76:79], a[44:47], v[56:59], v[76:79]\n"
"v_exp_f32 v43, v43\n"
"v_mfma_f32_16x16x32_bf16 v[112:115], a[44:47], v[60:63], v[112:115]\n"
"ds_read_b128 a[8:11], v6 offset:4096\n"
"v_mfma_f32_16x16x32_bf16 v[80:83], a[48:51], v[56:59], v[80:83]\n"
"ds_read_b128 a[12:15], v6 offset:6144\n"
"v_mfma_f32_16x16x32_bf16 v[116:119], a[48:51], v[60:63], v[116:119]\n"
"v_exp_f32 v44, v44\n"
"v_mfma_f32_16x16x32_bf16 v[84:87], a[52:55], v[56:59], v[84:87]\n"
"ds_read_b64 a[16:17], v7 offset:0\n"
"v_cvt_pk_bf16_f32 v65, v42, v43\n"
"v_mfma_f32_16x16x32_bf16 v[120:123], a[52:55], v[60:63], v[120:123]\n"
"v_exp_f32 v45, v45\n"
"v_mfma_f32_16x16x32_bf16 v[88:91], a[56:59], v[56:59], v[88:91]\n"
"v_exp_f32 v46, v46\n"
"v_mfma_f32_16x16x32_bf16 v[124:127], a[56:59], v[60:63], v[124:127]\n"
"ds_read_b128 a[20:23], v6 offset:512\n"
"v_cvt_pk_bf16_f32 v66, v44, v45\n"
"v_mfma_f32_16x16x32_bf16 v[92:95], a[60:63], v[56:59], v[92:95]\n"
"v_exp_f32 v47, v47\n"
"v_mfma_f32_16x16x32_bf16 v[128:131], a[60:63], v[60:63], v[128:131]\n"
"v_exp_f32 v48, v48\n"
"v_mfma_f32_16x16x32_bf16 v[96:99], a[64:67], v[56:59], v[96:99]\n"
"s_sub_u32 s46, s46, 1\n"
"ds_read_b128 a[24:27], v6 offset:2560\n"
"v_cvt_pk_bf16_f32 v67, v46, v47\n"
"v_mfma_f32_16x16x32_bf16 v[132:135], a[64:67], v[60:63], v[132:135]\n"
"s_waitcnt lgkmcnt(3)\n"
"s_cmp_eq_u32 s46, 0\n"
"ds_read_b128 a[28:31], v6 offset:4608\n"
"ds_read_b128 a[32:35], v6 offset:6656\n"
"v_mfma_f32_16x16x32_bf16 v[100:103], a[68:71], v[56:59], v[100:103]\n"
"v_exp_f32 v49, v49\n"
"v_mfma_f32_16x16x32_bf16 v[136:139], a[68:71], v[60:63], v[136:139]\n"
"ds_read_b64 a[36:37], v7 offset:512\n"
"v_mfma_f32_16x16x32_bf16 v[104:107], a[72:75], v[56:59], v[104:107]\n"
"v_mfma_f32_16x16x32_bf16 v[140:143], a[72:75], v[60:63], v[140:143]\n"
"s_cbranch_scc0 L_loop_%=\n"
"s_mov_b64 exec, 1\n"
"global_store_dword v220, v221, s[66:67] sc1\n"
"s_mov_b64 exec, -1\n"
"v_mfma_f32_16x16x32_bf16 v[24:27], a[0:3], a[76:79], 0\n"
"v_exp_f32 v50, v50\n"
"v_mfma_f32_16x16x32_bf16 v[24:27], a[4:7], a[80:83], v[24:27]\n"
"s_lshl_b32 s22, s77, 10\n"
"s_add_u32 m0, s45, 0\n"
"v_cvt_pk_bf16_f32 v68, v48, v49\n"
"v_mfma_f32_16x16x32_bf16 v[24:27], a[8:11], a[84:87], v[24:27]\n"
"s_and_b32 s51, s77, 1\n"
"v_exp_f32 v51, v51\n"
"v_mfma_f32_16x16x32_bf16 v[24:27], a[12:15], a[88:91], v[24:27]\n"
"s_waitcnt lgkmcnt(3)\n"
"s_mul_i32 s23, s51, 4608\n"
"v_mfma_f32_16x16x32_bf16 v[24:27], a[16:19], a[92:95], v[24:27]\n"
"s_add_u32 s23, s23, 4096\n"
"v_exp_f32 v52, v52\n"
"v_mfma_f32_16x16x32_bf16 v[28:31], a[20:23], a[76:79], 0\n"
"s_mul_i32 s84, s78, 9216\n"
"v_mfma_f32_16x16x32_bf16 v[28:31], a[24:27], a[80:83], v[28:31]\n"
"s_waitcnt lgkmcnt(0)\n"
"s_add_u32 s83, s84, s21\n"
"v_cvt_pk_bf16_f32 v69, v50, v51\n"
"v_mfma_f32_16x16x32_bf16 v[28:31], a[28:31], a[84:87], v[28:31]\n"
"s_mul_i32 s51, s51, 288\n"
"v_mfma_f32_16x16x32_bf16 v[28:31], a[32:35], a[88:91], v[28:31]\n"
"s_add_u32 s84, s84, s51\n"
"v_exp_f32 v24, v24\n"
"v_mfma_f32_16x16x32_bf16 v[28:31], a[36:39], a[92:95], v[28:31]\n"
"s_mov_b32 s62, 1152000\n"
"v_exp_f32 v53, v53\n"
"v_mfma_f32_16x16x32_bf16 v[32:35], a[0:3], a[96:99], 0\n"
"s_mov_b32 s63, 0x20000\n"
"v_exp_f32 v25, v25\n"
"v_mfma_f32_16x16x32_bf16 v[32:35], a[4:7], a[100:103], v[32:35]\n"
"buffer_load_dwordx4 v10, s[40:43], s44 offen offset:0 sc1 lds\n"
"v_mfma_f32_16x16x32_bf16 v[32:35], a[8:11], a[104:107], v[32:35]\n"
"buffer_load_dwordx4 v11, s[40:43], s44 offen offset:1024 sc1 lds\n"
"v_mfma_f32_16x16x32_bf16 v[32:35], a[12:15], a[108:111], v[32:35]\n"
"buffer_load_dwordx4 v12, s[40:43], s44 offen offset:2048 sc1 lds\n"
"v_mfma_f32_16x16x32_bf16 v[32:35], a[16:19], a[112:115], v[32:35]\n"
"v_exp_f32 v26, v26\n"
"v_mfma_f32_16x16x32_bf16 v[36:39], a[20:23], a[96:99], 0\n"
"v_cvt_pk_bf16_f32 v56, v24, v25\n"
"v_mfma_f32_16x16x32_bf16 v[36:39], a[24:27], a[100:103], v[36:39]\n"
"v_exp_f32 v27, v27\n"
"v_mfma_f32_16x16x32_bf16 v[36:39], a[28:31], a[104:107], v[36:39]\n"
"v_exp_f32 v54, v54\n"
"v_mfma_f32_16x16x32_bf16 v[36:39], a[32:35], a[108:111], v[36:39]\n"
"v_cvt_pk_bf16_f32 v70, v52, v53\n"
"v_exp_f32 v55, v55\n"
"v_exp_f32 v28, v28\n"
"v_mfma_f32_16x16x32_bf16 v[36:39], a[36:39], a[112:115], v[36:39]\n"
"buffer_load_dwordx4 v13, s[40:43], s44 offen offset:3072 sc1 lds\n"
"v_cvt_pk_bf16_f32 v71, v54, v55\n"
"v_mfma_f32_16x16x32_bf16 v[144:147], a[40:43], v[64:67], v[144:147]\n"
"v_cvt_pk_bf16_f32 v57, v26, v27\n"
"v_lshrrev_b32 v222, 2, v21\n"
"v_mfma_f32_16x16x32_bf16 v[180:183], a[40:43], v[68:71], v[180:183]\n"
"ds_read_b64_tr_b16 a[40:41], v8 offset:0\n"
"v_mfma_f32_16x16x32_bf16 v[148:151], a[44:47], v[64:67], v[148:151]\n"
"ds_read_b64_tr_b16 a[42:43], v8 offset:512\n"
"v_and_b32 v223, 1, v0\n"
"v_mfma_f32_16x16x32_bf16 v[184:187], a[44:47], v[68:71], v[184:187]\n"
"v_exp_f32 v29, v29\n"
"v_mfma_f32_16x16x32_bf16 v[152:155], a[48:51], v[64:67], v[152:155]\n"
"s_add_u32 m0, s45, 4096\n"
"v_exp_f32 v30, v30\n"
"v_mfma_f32_16x16x32_bf16 v[188:191], a[48:51], v[68:71], v[188:191]\n"
"buffer_load_dwordx4 v14, s[40:43], s44 offen offset:0 sc1 lds\n"
"v_mfma_f32_16x16x32_bf16 v[156:159], a[52:55], v[64:67], v[156:159]\n"
"ds_read_b64_tr_b16 a[44:45], v8 offset:1024\n"
"v_cvt_pk_bf16_f32 v58, v28, v29\n"
"v_mfma_f32_16x16x32_bf16 v[192:195], a[52:55], v[68:71], v[192:195]\n"
"ds_read_b64_tr_b16 a[46:47], v8 offset:1536\n"
"v_lshlrev_b32 v223, 4, v223\n"
"v_mfma_f32_16x16x32_bf16 v[160:163], a[56:59], v[64:67], v[160:163]\n"
"v_exp_f32 v31, v31\n"
"v_mfma_f32_16x16x32_bf16 v[196:199], a[56:59], v[68:71], v[196:199]\n"
"ds_read_b64_tr_b16 a[48:49], v8 offset:2048\n"
"v_mfma_f32_16x16x32_bf16 v[164:167], a[60:63], v[64:67], v[164:167]\n"
"buffer_load_dwordx4 v15, s[40:43], s44 offen offset:1024 sc1 lds\n"
"v_mfma_f32_16x16x32_bf16 v[200:203], a[60:63], v[68:71], v[200:203]\n"
"v_exp_f32 v32, v32\n"
"v_mfma_f32_16x16x32_bf16 v[168:171], a[64:67], v[64:67], v[168:171]\n"
"ds_read_b64_tr_b16 a[50:51], v8 offset:2560\n"
"v_cvt_pk_bf16_f32 v59, v30, v31\n"
"v_mfma_f32_16x16x32_bf16 v[204:207], a[64:67], v[68:71], v[204:207]\n"
"ds_read_b64_tr_b16 a[52:53], v8 offset:3072\n"
"ds_read_b64_tr_b16 a[54:55], v8 offset:3584\n"
"v_mfma_f32_16x16x32_bf16 v[172:175], a[68:71], v[64:67], v[172:175]\n"
"v_exp_f32 v33, v33\n"
"v_mfma_f32_16x16x32_bf16 v[208:211], a[68:71], v[68:71], v[208:211]\n"
"ds_read_b64_tr_b16 a[56:57], v8 offset:4096\n"
"v_mov_b32 v224, 0x3f80\n"
"v_mfma_f32_16x16x32_bf16 v[176:179], a[72:75], v[64:67], v[176:179]\n"
"buffer_load_dwordx4 v16, s[40:43], s44 offen offset:2048 sc1 lds\n"
"v_mfma_f32_16x16x32_bf16 v[212:215], a[72:75], v[68:71], v[212:215]\n"
"v_exp_f32 v34, v34\n"
"v_mfma_f32_16x16x32_bf16 v[40:43], a[0:3], a[116:119], 0\n"
"ds_read_b64_tr_b16 a[58:59], v8 offset:4608\n"
"v_cvt_pk_bf16_f32 v60, v32, v33\n"
"v_mfma_f32_16x16x32_bf16 v[40:43], a[4:7], a[120:123], v[40:43]\n"
"ds_read_b64_tr_b16 a[60:61], v8 offset:5120\n"
"v_lshlrev_b32 v224, v223, v224\n"
"v_mfma_f32_16x16x32_bf16 v[40:43], a[8:11], a[124:127], v[40:43]\n"
"v_exp_f32 v35, v35\n"
"v_mfma_f32_16x16x32_bf16 v[40:43], a[12:15], a[128:131], v[40:43]\n"
"ds_read_b64_tr_b16 a[62:63], v8 offset:5632\n"
"v_cmp_eq_u32 s[16:17], v222, v22\n"
"v_mfma_f32_16x16x32_bf16 v[40:43], a[16:19], a[132:135], v[40:43]\n"
"buffer_load_dwordx4 v17, s[40:43], s44 offen offset:3072 sc1 lds\n"
"v_mfma_f32_16x16x32_bf16 v[44:47], a[20:23], a[116:119], 0\n"
"v_exp_f32 v36, v36\n"
"v_mfma_f32_16x16x32_bf16 v[44:47], a[24:27], a[120:123], v[44:47]\n"
"ds_read_b64_tr_b16 a[64:65], v8 offset:6144\n"
"v_cvt_pk_bf16_f32 v61, v34, v35\n"
"v_mfma_f32_16x16x32_bf16 v[44:47], a[28:31], a[124:127], v[44:47]\n"
"ds_read_b64_tr_b16 a[66:67], v8 offset:6656\n"
"v_and_b32 v223, 2, v0\n"
"v_mfma_f32_16x16x32_bf16 v[44:47], a[32:35], a[128:131], v[44:47]\n"
"v_exp_f32 v37, v37\n"
"v_mfma_f32_16x16x32_bf16 v[44:47], a[36:39], a[132:135], v[44:47]\n"
"s_add_u32 m0, s45, 8192\n"
"ds_read_b64_tr_b16 a[68:69], v8 offset:7168\n"
"v_cmp_eq_u32 s[18:19], 0, v223\n"
"v_mfma_f32_16x16x32_bf16 v[48:51], a[0:3], a[136:139], 0\n"
"v_exp_f32 v38, v38\n"
"v_mfma_f32_16x16x32_bf16 v[48:51], a[4:7], a[140:143], v[48:51]\n"
"buffer_load_dwordx4 v18, s[40:43], s44 offen offset:0 sc1 lds\n"
"v_mfma_f32_16x16x32_bf16 v[48:51], a[8:11], a[144:147], v[48:51]\n"
"ds_read_b64_tr_b16 a[70:71], v8 offset:7680\n"
"v_cvt_pk_bf16_f32 v62, v36, v37\n"
"v_mfma_f32_16x16x32_bf16 v[48:51], a[12:15], a[148:151], v[48:51]\n"
"ds_read_b64_tr_b16 a[72:73], v9 offset:8192\n"
"v_add_u32 v241, s45, v23\n"
"v_mfma_f32_16x16x32_bf16 v[48:51], a[16:19], a[152:155], v[48:51]\n"
"v_exp_f32 v39, v39\n"
"v_mfma_f32_16x16x32_bf16 v[52:55], a[20:23], a[136:139], 0\n"
"ds_read_b64_tr_b16 a[74:75], v9 offset:8704\n"
"v_cndmask_b32 v224, 0, v224, s[16:17]\n"
"v_mfma_f32_16x16x32_bf16 v[52:55], a[24:27], a[140:143], v[52:55]\n"
"v_cvt_pk_bf16_f32 v63, v38, v39\n"
"v_mfma_f32_16x16x32_bf16 v[52:55], a[28:31], a[144:147], v[52:55]\n"
"v_exp_f32 v40, v40\n"
"v_mfma_f32_16x16x32_bf16 v[52:55], a[32:35], a[148:151], v[52:55]\n"
"v_exp_f32 v41, v41\n"
"v_mfma_f32_16x16x32_bf16 v[52:55], a[36:39], a[152:155], v[52:55]\n"
"s_waitcnt vmcnt(10) lgkmcnt(3)\n"
"v_exp_f32 v42, v42\n"
"v_mfma_f32_16x16x32_bf16 v[72:75], a[40:43], v[56:59], v[72:75]\n"
"ds_read_b128 a[0:3], v2 offset:36864\n"
"v_mul_u32_u24 v254, 576, v21\n"
"v_mfma_f32_16x16x32_bf16 v[108:111], a[40:43], v[60:63], v[108:111]\n"
"ds_read_b128 a[4:7], v2 offset:38912\n"
"v_cvt_pk_bf16_f32 v64, v40, v41\n"
"v_mfma_f32_16x16x32_bf16 v[76:79], a[44:47], v[56:59], v[76:79]\n"
"v_exp_f32 v43, v43\n"
"v_mfma_f32_16x16x32_bf16 v[112:115], a[44:47], v[60:63], v[112:115]\n"
"ds_read_b128 a[8:11], v2 offset:40960\n"
"v_cndmask_b32 v222, 0, v224, s[18:19]\n"
"v_mfma_f32_16x16x32_bf16 v[80:83], a[48:51], v[56:59], v[80:83]\n"
"ds_read_b128 a[12:15], v2 offset:43008\n"
"v_mfma_f32_16x16x32_bf16 v[116:119], a[48:51], v[60:63], v[116:119]\n"
"v_exp_f32 v44, v44\n"
"v_mfma_f32_16x16x32_bf16 v[84:87], a[52:55], v[56:59], v[84:87]\n"
"ds_read_b64 a[16:17], v3 offset:36864\n"
"v_cvt_pk_bf16_f32 v65, v42, v43\n"
"v_mfma_f32_16x16x32_bf16 v[120:123], a[52:55], v[60:63], v[120:123]\n"
"v_exp_f32 v45, v45\n"
"v_mfma_f32_16x16x32_bf16 v[88:91], a[56:59], v[56:59], v[88:91]\n"
"v_exp_f32 v46, v46\n"
"v_mfma_f32_16x16x32_bf16 v[124:127], a[56:59], v[60:63], v[124:127]\n"
"ds_read_b128 a[20:23], v2 offset:37376\n"
"v_cvt_pk_bf16_f32 v66, v44, v45\n"
"v_mfma_f32_16x16x32_bf16 v[92:95], a[60:63], v[56:59], v[92:95]\n"
"v_exp_f32 v47, v47\n"
"v_mfma_f32_16x16x32_bf16 v[128:131], a[60:63], v[60:63], v[128:131]\n"
"v_exp_f32 v48, v48\n"
"v_mfma_f32_16x16x32_bf16 v[96:99], a[64:67], v[56:59], v[96:99]\n"
"ds_read_b128 a[24:27], v2 offset:39424\n"
"v_cvt_pk_bf16_f32 v67, v46, v47\n"
"v_mfma_f32_16x16x32_bf16 v[132:135], a[64:67], v[60:63], v[132:135]\n"
"s_waitcnt lgkmcnt(3)\n"
"ds_read_b128 a[28:31], v2 offset:41472\n"
"ds_read_b128 a[32:35], v2 offset:43520\n"
"v_mfma_f32_16x16x32_bf16 v[100:103], a[68:71], v[56:59], v[100:103]\n"
"v_exp_f32 v49, v49\n"
"v_mfma_f32_16x16x32_bf16 v[136:139], a[68:71], v[60:63], v[136:139]\n"
"ds_read_b64 a[36:37], v3 offset:37376\n"
"v_cndmask_b32 v223, v224, 0, s[18:19]\n"
"v_mfma_f32_16x16x32_bf16 v[104:107], a[72:75], v[56:59], v[104:107]\n"
"v_accvgpr_write_b32 a196, 0\n"
"v_mfma_f32_16x16x32_bf16 v[140:143], a[72:75], v[60:63], v[140:143]\n"
"v_mfma_f32_16x16x32_bf16 v[24:27], a[0:3], a[76:79], 0\n"
"global_load_dword v20, v19, s[66:67] sc1\n"
"v_mfma_f32_16x16x32_bf16 v[24:27], a[4:7], a[80:83], v[24:27]\n"
"s_add_u32 s44, s21, 0\n"
"s_add_u32 m0, s45, 73728\n"
"v_exp_f32 v50, v50\n"
"v_mfma_f32_16x16x32_bf16 v[24:27], a[8:11], a[84:87], v[24:27]\n"
"buffer_load_dwordx4 v252, s[28:31], s44 offen offset:0 lds\n"
"v_mfma_f32_16x16x32_bf16 v[24:27], a[12:15], a[88:91], v[24:27]\n"
"s_waitcnt lgkmcnt(3)\n"
"v_cvt_pk_bf16_f32 v68, v48, v49\n"
"v_mfma_f32_16x16x32_bf16 v[24:27], a[16:19], a[92:95], v[24:27]\n"
"v_exp_f32 v51, v51\n"
"v_mfma_f32_16x16x32_bf16 v[28:31], a[20:23], a[76:79], 0\n"
"v_exp_f32 v52, v52\n"
"v_mfma_f32_16x16x32_bf16 v[28:31], a[24:27], a[80:83], v[28:31]\n"
"s_waitcnt lgkmcnt(0)\n"
"s_add_u32 s55, s21, 8192\n"
"v_cvt_pk_bf16_f32 v69, v50, v51\n"
"v_mfma_f32_16x16x32_bf16 v[28:31], a[28:31], a[84:87], v[28:31]\n"
"buffer_load_dwordx4 v252, s[28:31], s55 offen offset:1024 lds\n"
"v_mfma_f32_16x16x32_bf16 v[28:31], a[32:35], a[88:91], v[28:31]\n"
"v_exp_f32 v24, v24\n"
"v_mfma_f32_16x16x32_bf16 v[28:31], a[36:39], a[92:95], v[28:31]\n"
"v_exp_f32 v53, v53\n"
"v_mfma_f32_16x16x32_bf16 v[32:35], a[0:3], a[96:99], 0\n"
"v_exp_f32 v25, v25\n"
"v_mfma_f32_16x16x32_bf16 v[32:35], a[4:7], a[100:103], v[32:35]\n"
"v_exp_f32 v54, v54\n"
"v_mfma_f32_16x16x32_bf16 v[32:35], a[8:11], a[104:107], v[32:35]\n"
"s_add_u32 s44, s21, 16384\n"
"v_exp_f32 v26, v26\n"
"v_mfma_f32_16x16x32_bf16 v[32:35], a[12:15], a[108:111], v[32:35]\n"
"buffer_load_dwordx4 v252, s[28:31], s44 offen offset:2048 lds\n"
"v_mfma_f32_16x16x32_bf16 v[32:35], a[16:19], a[112:115], v[32:35]\n"
"v_cvt_pk_bf16_f32 v70, v52, v53\n"
"v_cvt_pk_bf16_f32 v56, v24, v25\n"
"v_mfma_f32_16x16x32_bf16 v[36:39], a[20:23], a[96:99], 0\n"
"v_exp_f32 v55, v55\n"
"v_mfma_f32_16x16x32_bf16 v[36:39], a[24:27], a[100:103], v[36:39]\n"
"v_exp_f32 v27, v27\n"
"v_mfma_f32_16x16x32_bf16 v[36:39], a[28:31], a[104:107], v[36:39]\n"
"v_cvt_pk_bf16_f32 v71, v54, v55\n"
"v_mfma_f32_16x16x32_bf16 v[36:39], a[32:35], a[108:111], v[36:39]\n"
"s_add_u32 s55, s21, 24576\n"
"v_exp_f32 v28, v28\n"
"v_mfma_f32_16x16x32_bf16 v[36:39], a[36:39], a[112:115], v[36:39]\n"
"buffer_load_dwordx4 v252, s[28:31], s55 offen offset:3072 lds\n"
"v_mfma_f32_16x16x32_bf16 v[144:147], a[40:43], v[64:67], v[144:147]\n"
"v_cvt_pk_bf16_f32 v57, v26, v27\n"
"v_accvgpr_write_b32 a197, 0\n"
"v_mfma_f32_16x16x32_bf16 v[180:183], a[40:43], v[68:71], v[180:183]\n"
"ds_read_b64_tr_b16 a[40:41], v4 offset:36864\n"
"v_mfma_f32_16x16x32_bf16 v[148:151], a[44:47], v[64:67], v[148:151]\n"
"ds_read_b64_tr_b16 a[42:43], v4 offset:37376\n"
"v_accvgpr_write_b32 a198, v222\n"
"v_mfma_f32_16x16x32_bf16 v[184:187], a[44:47], v[68:71], v[184:187]\n"
"v_exp_f32 v29, v29\n"
"v_mfma_f32_16x16x32_bf16 v[152:155], a[48:51], v[64:67], v[152:155]\n"
"s_add_u32 s44, s21, 36864\n"
"s_add_u32 m0, s45, 77824\n"
"v_exp_f32 v30, v30\n"
"v_mfma_f32_16x16x32_bf16 v[188:191], a[48:51], v[68:71], v[188:191]\n"
"buffer_load_dwordx4 v252, s[28:31], s44 offen offset:0 lds\n"
"v_mfma_f32_16x16x32_bf16 v[156:159], a[52:55], v[64:67], v[156:159]\n"
"ds_read_b64_tr_b16 a[44:45], v4 offset:37888\n"
"v_cvt_pk_bf16_f32 v58, v28, v29\n"
"v_mfma_f32_16x16x32_bf16 v[192:195], a[52:55], v[68:71], v[192:195]\n"
"ds_read_b64_tr_b16 a[46:47], v4 offset:38400\n"
"v_accvgpr_write_b32 a199, v223\n"
"v_mfma_f32_16x16x32_bf16 v[160:163], a[56:59], v[64:67], v[160:163]\n"
"v_exp_f32 v31, v31\n"
"v_mfma_f32_16x16x32_bf16 v[196:199], a[56:59], v[68:71], v[196:199]\n"
"s_add_u32 s55, s21, 45056\n"
"ds_read_b64_tr_b16 a[48:49], v4 offset:38912\n"
"v_accvgpr_write_b32 a200, 0\n"
"v_mfma_f32_16x16x32_bf16 v[164:167], a[60:63], v[64:67], v[164:167]\n"
"buffer_load_dwordx4 v252, s[28:31], s55 offen offset:1024 lds\n"
"v_mfma_f32_16x16x32_bf16 v[200:203], a[60:63], v[68:71], v[200:203]\n"
"v_exp_f32 v32, v32\n"
"v_mfma_f32_16x16x32_bf16 v[168:171], a[64:67], v[64:67], v[168:171]\n"
"ds_read_b64_tr_b16 a[50:51], v4 offset:39424\n"
"v_cvt_pk_bf16_f32 v59, v30, v31\n"
"v_mfma_f32_16x16x32_bf16 v[204:207], a[64:67], v[68:71], v[204:207]\n"
"ds_read_b64_tr_b16 a[52:53], v4 offset:39936\n"
"ds_read_b64_tr_b16 a[54:55], v4 offset:40448\n"
"v_mfma_f32_16x16x32_bf16 v[172:175], a[68:71], v[64:67], v[172:175]\n"
"v_exp_f32 v33, v33\n"
"v_mfma_f32_16x16x32_bf16 v[208:211], a[68:71], v[68:71], v[208:211]\n"
"s_add_u32 s44, s21, 53248\n"
"ds_read_b64_tr_b16 a[56:57], v4 offset:40960\n"
"v_accvgpr_write_b32 a201, 0\n"
"v_mfma_f32_16x16x32_bf16 v[176:179], a[72:75], v[64:67], v[176:179]\n"
"buffer_load_dwordx4 v252, s[28:31], s44 offen offset:2048 lds\n"
"v_mfma_f32_16x16x32_bf16 v[212:215], a[72:75], v[68:71], v[212:215]\n"
"v_exp_f32 v34, v34\n"
"v_mfma_f32_16x16x32_bf16 v[40:43], a[0:3], a[116:119], 0\n"
"ds_read_b64_tr_b16 a[58:59], v4 offset:41472\n"
"v_cvt_pk_bf16_f32 v60, v32, v33\n"
"v_mfma_f32_16x16x32_bf16 v[40:43], a[4:7], a[120:123], v[40:43]\n"
"ds_read_b64_tr_b16 a[60:61], v4 offset:41984\n"
"v_accvgpr_write_b32 a202, v222\n"
"v_mfma_f32_16x16x32_bf16 v[40:43], a[8:11], a[124:127], v[40:43]\n"
"v_exp_f32 v35, v35\n"
"v_mfma_f32_16x16x32_bf16 v[40:43], a[12:15], a[128:131], v[40:43]\n"
"s_add_u32 s55, s21, 61440\n"
"ds_read_b64_tr_b16 a[62:63], v4 offset:42496\n"
"v_accvgpr_write_b32 a203, v223\n"
"v_mfma_f32_16x16x32_bf16 v[40:43], a[16:19], a[132:135], v[40:43]\n"
"buffer_load_dwordx4 v252, s[28:31], s55 offen offset:3072 lds\n"
"v_mfma_f32_16x16x32_bf16 v[44:47], a[20:23], a[116:119], 0\n"
"v_exp_f32 v36, v36\n"
"v_mfma_f32_16x16x32_bf16 v[44:47], a[24:27], a[120:123], v[44:47]\n"
"ds_read_b64_tr_b16 a[64:65], v4 offset:43008\n"
"v_cvt_pk_bf16_f32 v61, v34, v35\n"
"v_mfma_f32_16x16x32_bf16 v[44:47], a[28:31], a[124:127], v[44:47]\n"
"ds_read_b64_tr_b16 a[66:67], v4 offset:43520\n"
"v_accvgpr_write_b32 a204, v222\n"
"v_mfma_f32_16x16x32_bf16 v[44:47], a[32:35], a[128:131], v[44:47]\n"
"v_exp_f32 v37, v37\n"
"v_mfma_f32_16x16x32_bf16 v[44:47], a[36:39], a[132:135], v[44:47]\n"
"s_add_u32 m0, s45, 81920\n"
"ds_read_b64_tr_b16 a[68:69], v4 offset:44032\n"
"v_accvgpr_write_b32 a205, v223\n"
"v_mfma_f32_16x16x32_bf16 v[48:51], a[0:3], a[136:139], 0\n"
"s_add_u32 s44, s21, 73728\n"
"v_exp_f32 v38, v38\n"
"v_mfma_f32_16x16x32_bf16 v[48:51], a[4:7], a[140:143], v[48:51]\n"
"buffer_load_dwordx4 v252, s[28:31], s44 offen offset:0 lds\n"
"v_mfma_f32_16x16x32_bf16 v[48:51], a[8:11], a[144:147], v[48:51]\n"
"ds_read_b64_tr_b16 a[70:71], v4 offset:44544\n"
"v_cvt_pk_bf16_f32 v62, v36, v37\n"
"v_mfma_f32_16x16x32_bf16 v[48:51], a[12:15], a[148:151], v[48:51]\n"
"ds_read_b64_tr_b16 a[72:73], v5 offset:45056\n"
"v_lshl_add_u32 v255, v22, 3, v254\n"
"v_mfma_f32_16x16x32_bf16 v[48:51], a[16:19], a[152:155], v[48:51]\n"
"v_exp_f32 v39, v39\n"
"v_mfma_f32_16x16x32_bf16 v[52:55], a[20:23], a[136:139], 0\n"
"ds_read_b64_tr_b16 a[74:75], v5 offset:45568\n"
"v_lshl_add_u32 v254, v22, 4, v254\n"
"v_mfma_f32_16x16x32_bf16 v[52:55], a[24:27], a[140:143], v[52:55]\n"
"v_cvt_pk_bf16_f32 v63, v38, v39\n"
"v_add_u32 v237, 0x1000, v238\n"
"v_mfma_f32_16x16x32_bf16 v[52:55], a[28:31], a[144:147], v[52:55]\n"
"v_exp_f32 v40, v40\n"
"v_mfma_f32_16x16x32_bf16 v[52:55], a[32:35], a[148:151], v[52:55]\n"
"v_exp_f32 v41, v41\n"
"v_mfma_f32_16x16x32_bf16 v[52:55], a[36:39], a[152:155], v[52:55]\n"
"s_waitcnt vmcnt(9) lgkmcnt(3)\n"
"v_exp_f32 v42, v42\n"
"v_mfma_f32_16x16x32_bf16 v[72:75], a[40:43], v[56:59], v[72:75]\n"
"ds_read_b128 a[0:3], v2 offset:0\n"
"v_cmp_eq_u32 vcc, s49, v20\n"
"v_mfma_f32_16x16x32_bf16 v[108:111], a[40:43], v[60:63], v[108:111]\n"
"ds_read_b128 a[4:7], v2 offset:2048\n"
"v_cvt_pk_bf16_f32 v64, v40, v41\n"
"v_mfma_f32_16x16x32_bf16 v[76:79], a[44:47], v[56:59], v[76:79]\n"
"v_exp_f32 v43, v43\n"
"v_mfma_f32_16x16x32_bf16 v[112:115], a[44:47], v[60:63], v[112:115]\n"
"s_and_b32 s48, vcc_lo, vcc_hi\n"
"ds_read_b128 a[8:11], v2 offset:4096\n"
"v_accvgpr_write_b32 a244, 0\n"
"v_mfma_f32_16x16x32_bf16 v[80:83], a[48:51], v[56:59], v[80:83]\n"
"ds_read_b128 a[12:15], v2 offset:6144\n"
"v_accvgpr_write_b32 a245, 0\n"
"v_mfma_f32_16x16x32_bf16 v[116:119], a[48:51], v[60:63], v[116:119]\n"
"v_exp_f32 v44, v44\n"
"v_mfma_f32_16x16x32_bf16 v[84:87], a[52:55], v[56:59], v[84:87]\n"
"ds_read_b64 a[16:17], v3 offset:0\n"
"v_cvt_pk_bf16_f32 v65, v42, v43\n"
"v_mfma_f32_16x16x32_bf16 v[120:123], a[52:55], v[60:63], v[120:123]\n"
"v_exp_f32 v45, v45\n"
"v_mfma_f32_16x16x32_bf16 v[88:91], a[56:59], v[56:59], v[88:91]\n"
"v_exp_f32 v46, v46\n"
"v_mfma_f32_16x16x32_bf16 v[124:127], a[56:59], v[60:63], v[124:127]\n"
"ds_read_b128 a[20:23], v2 offset:512\n"
"v_cvt_pk_bf16_f32 v66, v44, v45\n"
"v_mfma_f32_16x16x32_bf16 v[92:95], a[60:63], v[56:59], v[92:95]\n"
"v_exp_f32 v47, v47\n"
"v_mfma_f32_16x16x32_bf16 v[128:131], a[60:63], v[60:63], v[128:131]\n"
"v_exp_f32 v48, v48\n"
"v_mfma_f32_16x16x32_bf16 v[96:99], a[64:67], v[56:59], v[96:99]\n"
"ds_read_b128 a[24:27], v2 offset:2560\n"
"v_cvt_pk_bf16_f32 v67, v46, v47\n"
"v_mfma_f32_16x16x32_bf16 v[132:135], a[64:67], v[60:63], v[132:135]\n"
"s_waitcnt lgkmcnt(3)\n"
"ds_read_b128 a[28:31], v2 offset:4608\n"
"ds_read_b128 a[32:35], v2 offset:6656\n"
"v_mfma_f32_16x16x32_bf16 v[100:103], a[68:71], v[56:59], v[100:103]\n"
"v_exp_f32 v49, v49\n"
"v_mfma_f32_16x16x32_bf16 v[136:139], a[68:71], v[60:63], v[136:139]\n"
"ds_read_b64 a[36:37], v3 offset:512\n"
"v_add_u32 v242, s22, v23\n"
"v_mfma_f32_16x16x32_bf16 v[104:107], a[72:75], v[56:59], v[104:107]\n"
"v_add_u32 v246, s23, v23\n"
"v_mfma_f32_16x16x32_bf16 v[140:143], a[72:75], v[60:63], v[140:143]\n"
"s_lshr_b32 s50, s48, 17\n"
"s_andn2_b32 s50, 15, s50\n"
"s_cbranch_scc1 L_phs_%=\n"
"L_phr_%=:\n"
"v_mfma_f32_16x16x32_bf16 v[24:27], a[0:3], a[76:79], 0\n"
"v_exp_f32 v50, v50\n"
"v_mfma_f32_16x16x32_bf16 v[24:27], a[4:7], a[80:83], v[24:27]\n"
"v_cvt_pk_bf16_f32 v68, v48, v49\n"
"v_mfma_f32_16x16x32_bf16 v[24:27], a[8:11], a[84:87], v[24:27]\n"
"v_exp_f32 v51, v51\n"
"v_mfma_f32_16x16x32_bf16 v[24:27], a[12:15], a[88:91], v[24:27]\n"
"s_waitcnt lgkmcnt(3)\n"
"buffer_load_dwordx4 a[156:159], v238, s[56:59], s87 offen offset:0 sc1\n"
"v_mfma_f32_16x16x32_bf16 v[24:27], a[16:19], a[92:95], v[24:27]\n"
"v_exp_f32 v52, v52\n"
"v_mfma_f32_16x16x32_bf16 v[28:31], a[20:23], a[76:79], 0\n"
"v_mfma_f32_16x16x32_bf16 v[28:31], a[24:27], a[80:83], v[28:31]\n"
"s_waitcnt lgkmcnt(0)\n"
"v_cvt_pk_bf16_f32 v69, v50, v51\n"
"v_mfma_f32_16x16x32_bf16 v[28:31], a[28:31], a[84:87], v[28:31]\n"
"buffer_load_dwordx4 a[160:163], v238, s[56:59], s87 offen offset:1024 sc1\n"
"v_mfma_f32_16x16x32_bf16 v[28:31], a[32:35], a[88:91], v[28:31]\n"
"v_exp_f32 v24, v24\n"
"v_mfma_f32_16x16x32_bf16 v[28:31], a[36:39], a[92:95], v[28:31]\n"
"v_exp_f32 v53, v53\n"
"v_mfma_f32_16x16x32_bf16 v[32:35], a[0:3], a[96:99], 0\n"
"v_exp_f32 v25, v25\n"
"v_mfma_f32_16x16x32_bf16 v[32:35], a[4:7], a[100:103], v[32:35]\n"
"buffer_load_dwordx4 a[164:167], v238, s[56:59], s87 offen offset:2048 sc1\n"
"v_mfma_f32_16x16x32_bf16 v[32:35], a[8:11], a[104:107], v[32:35]\n"
"v_exp_f32 v26, v26\n"
"v_mfma_f32_16x16x32_bf16 v[32:35], a[12:15], a[108:111], v[32:35]\n"
"v_exp_f32 v54, v54\n"
"v_mfma_f32_16x16x32_bf16 v[32:35], a[16:19], a[112:115], v[32:35]\n"
"v_cvt_pk_bf16_f32 v70, v52, v53\n"
"v_cvt_pk_bf16_f32 v56, v24, v25\n"
"v_mfma_f32_16x16x32_bf16 v[36:39], a[20:23], a[96:99], 0\n"
"buffer_load_dwordx4 a[168:171], v238, s[56:59], s87 offen offset:3072 sc1\n"
"v_mfma_f32_16x16x32_bf16 v[36:39], a[24:27], a[100:103], v[36:39]\n"
"v_exp_f32 v27, v27\n"
"v_mfma_f32_16x16x32_bf16 v[36:39], a[28:31], a[104:107], v[36:39]\n"
"v_exp_f32 v55, v55\n"
"v_mfma_f32_16x16x32_bf16 v[36:39], a[32:35], a[108:111], v[36:39]\n"
"v_exp_f32 v28, v28\n"
"v_mfma_f32_16x16x32_bf16 v[36:39], a[36:39], a[112:115], v[36:39]\n"
"v_cvt_pk_bf16_f32 v71, v54, v55\n"
"v_mfma_f32_16x16x32_bf16 v[144:147], a[40:43], v[64:67], v[144:147]\n"
"v_cvt_pk_bf16_f32 v57, v26, v27\n"
"v_mfma_f32_16x16x32_bf16 v[180:183], a[40:43], v[68:71], v[180:183]\n"
"ds_read_b64_tr_b16 a[40:41], v4 offset:0\n"
"v_mfma_f32_16x16x32_bf16 v[148:151], a[44:47], v[64:67], v[148:151]\n"
"ds_read_b64_tr_b16 a[42:43], v4 offset:512\n"
"v_mfma_f32_16x16x32_bf16 v[184:187], a[44:47], v[68:71], v[184:187]\n"
"v_exp_f32 v29, v29\n"
"v_mfma_f32_16x16x32_bf16 v[152:155], a[48:51], v[64:67], v[152:155]\n"
"v_exp_f32 v30, v30\n"
"v_mfma_f32_16x16x32_bf16 v[188:191], a[48:51], v[68:71], v[188:191]\n"
"ds_read_b64_tr_b16 a[44:45], v4 offset:1024\n"
"v_mfma_f32_16x16x32_bf16 v[156:159], a[52:55], v[64:67], v[156:159]\n"
"ds_read_b64_tr_b16 a[46:47], v4 offset:1536\n"
"v_cvt_pk_bf16_f32 v58, v28, v29\n"
"v_mfma_f32_16x16x32_bf16 v[192:195], a[52:55], v[68:71], v[192:195]\n"
"buffer_load_dwordx4 a[172:175], v237, s[56:59], s87 offen offset:0 sc1\n"
"v_mfma_f32_16x16x32_bf16 v[160:163], a[56:59], v[64:67], v[160:163]\n"
"v_exp_f32 v31, v31\n"
"v_mfma_f32_16x16x32_bf16 v[196:199], a[56:59], v[68:71], v[196:199]\n"
"ds_read_b64_tr_b16 a[48:49], v4 offset:2048\n"
"v_mfma_f32_16x16x32_bf16 v[164:167], a[60:63], v[64:67], v[164:167]\n"
"global_load_dword v20, v19, s[66:67] sc1\n"
"v_mfma_f32_16x16x32_bf16 v[200:203], a[60:63], v[68:71], v[200:203]\n"
"v_exp_f32 v32, v32\n"
"v_mfma_f32_16x16x32_bf16 v[168:171], a[64:67], v[64:67], v[168:171]\n"
"ds_read_b64_tr_b16 a[50:51], v4 offset:2560\n"
"v_cvt_pk_bf16_f32 v59, v30, v31\n"
"v_mfma_f32_16x16x32_bf16 v[204:207], a[64:67], v[68:71], v[204:207]\n"
"buffer_load_dwordx4 a[176:179], v238, s[56:59], s52 offen offset:0 sc1\n"
"v_mfma_f32_16x16x32_bf16 v[172:175], a[68:71], v[64:67], v[172:175]\n"
"v_exp_f32 v33, v33\n"
"v_mfma_f32_16x16x32_bf16 v[208:211], a[68:71], v[68:71], v[208:211]\n"
"ds_read_b64_tr_b16 a[52:53], v4 offset:3072\n"
"ds_read_b64_tr_b16 a[54:55], v4 offset:3584\n"
"v_mfma_f32_16x16x32_bf16 v[176:179], a[72:75], v[64:67], v[176:179]\n"
"v_exp_f32 v34, v34\n"
"v_mfma_f32_16x16x32_bf16 v[212:215], a[72:75], v[68:71], v[212:215]\n"
"buffer_load_dwordx4 a[180:183], v238, s[56:59], s52 offen offset:1024 sc1\n"
"v_mfma_f32_16x16x32_bf16 v[40:43], a[0:3], a[116:119], 0\n"
"ds_read_b64_tr_b16 a[56:57], v4 offset:4096\n"
"v_cvt_pk_bf16_f32 v60, v32, v33\n"
"v_mfma_f32_16x16x32_bf16 v[40:43], a[4:7], a[120:123], v[40:43]\n"
"ds_read_b64_tr_b16 a[58:59], v4 offset:4608\n"
"ds_read_b64_tr_b16 a[60:61], v4 offset:5120\n"
"v_mfma_f32_16x16x32_bf16 v[40:43], a[8:11], a[124:127], v[40:43]\n"
"v_exp_f32 v35, v35\n"
"v_mfma_f32_16x16x32_bf16 v[40:43], a[12:15], a[128:131], v[40:43]\n"
"buffer_load_dwordx4 a[184:187], v238, s[56:59], s52 offen offset:2048 sc1\n"
"v_mfma_f32_16x16x32_bf16 v[40:43], a[16:19], a[132:135], v[40:43]\n"
"v_exp_f32 v36, v36\n"
"v_mfma_f32_16x16x32_bf16 v[44:47], a[20:23], a[116:119], 0\n"
"ds_read_b64_tr_b16 a[62:63], v4 offset:5632\n"
"ds_read_b64_tr_b16 a[64:65], v4 offset:6144\n"
"v_mfma_f32_16x16x32_bf16 v[44:47], a[24:27], a[120:123], v[44:47]\n"
"ds_read_b64_tr_b16 a[66:67], v4 offset:6656\n"
"v_cvt_pk_bf16_f32 v61, v34, v35\n"
"v_mfma_f32_16x16x32_bf16 v[44:47], a[28:31], a[124:127], v[44:47]\n"
"buffer_load_dwordx4 a[188:191], v238, s[56:59], s52 offen offset:3072 sc1\n"
"v_mfma_f32_16x16x32_bf16 v[44:47], a[32:35], a[128:131], v[44:47]\n"
"v_exp_f32 v37, v37\n"
"v_mfma_f32_16x16x32_bf16 v[44:47], a[36:39], a[132:135], v[44:47]\n"
"buffer_load_dwordx4 a[192:195], v237, s[56:59], s52 offen offset:0 sc1\n"
"v_mfma_f32_16x16x32_bf16 v[48:51], a[0:3], a[136:139], 0\n"
"v_exp_f32 v38, v38\n"
"v_mfma_f32_16x16x32_bf16 v[48:51], a[4:7], a[140:143], v[48:51]\n"
"ds_read_b64_tr_b16 a[68:69], v4 offset:7168\n"
"ds_read_b64_tr_b16 a[70:71], v4 offset:7680\n"
"v_mfma_f32_16x16x32_bf16 v[48:51], a[8:11], a[144:147], v[48:51]\n"
"ds_read_b64_tr_b16 a[72:73], v5 offset:8192\n"
"v_cvt_pk_bf16_f32 v62, v36, v37\n"
"v_mfma_f32_16x16x32_bf16 v[48:51], a[12:15], a[148:151], v[48:51]\n"
"buffer_load_dwordx4 v[216:219], v240, s[24:27], s21 offen offset:0\n"
"v_mfma_f32_16x16x32_bf16 v[48:51], a[16:19], a[152:155], v[48:51]\n"
"v_exp_f32 v39, v39\n"
"v_mfma_f32_16x16x32_bf16 v[52:55], a[20:23], a[136:139], 0\n"
"ds_read_b64_tr_b16 a[74:75], v5 offset:8704\n"
"v_mfma_f32_16x16x32_bf16 v[52:55], a[24:27], a[140:143], v[52:55]\n"
"v_cvt_pk_bf16_f32 v63, v38, v39\n"
"v_mfma_f32_16x16x32_bf16 v[52:55], a[28:31], a[144:147], v[52:55]\n"
"buffer_load_dwordx4 v[220:223], v240, s[24:27], s21 offen offset:288\n"
"v_mfma_f32_16x16x32_bf16 v[52:55], a[32:35], a[148:151], v[52:55]\n"
"v_exp_f32 v40, v40\n"
"v_mfma_f32_16x16x32_bf16 v[52:55], a[36:39], a[152:155], v[52:55]\n"
"s_waitcnt lgkmcnt(4)\n"
"v_exp_f32 v41, v41\n"
"v_mfma_f32_16x16x32_bf16 v[72:75], a[40:43], v[56:59], v[72:75]\n"
"buffer_load_dwordx4 v[224:227], v240, s[24:27], s21 offen offset:576\n"
"v_mfma_f32_16x16x32_bf16 v[76:79], a[44:47], v[56:59], v[76:79]\n"
"buffer_load_dwordx2 v[228:229], v239, s[24:27], 0 offen offset:256\n"
"v_mfma_f32_16x16x32_bf16 v[80:83], a[48:51], v[56:59], v[80:83]\n"
"s_nop 2\n"
"v_cvt_pk_bf16_f32 v72, v72, v73\n"
"v_mfma_f32_16x16x32_bf16 v[84:87], a[52:55], v[56:59], v[84:87]\n"
"buffer_load_dwordx2 v[230:231], v239, s[24:27], 0 offen offset:544\n"
"v_mfma_f32_16x16x32_bf16 v[88:91], a[56:59], v[56:59], v[88:91]\n"
"v_cvt_pk_bf16_f32 v73, v74, v75\n"
"v_cvt_pk_bf16_f32 v74, v76, v77\n"
"v_mfma_f32_16x16x32_bf16 v[92:95], a[60:63], v[56:59], v[92:95]\n"
"buffer_load_dwordx2 v[232:233], v239, s[24:27], 0 offen offset:832\n"
"v_mfma_f32_16x16x32_bf16 v[96:99], a[64:67], v[56:59], v[96:99]\n"
"ds_write_b64 v241, v[72:73] offset:36864\n"
"v_cvt_pk_bf16_f32 v75, v78, v79\n"
"s_waitcnt lgkmcnt(3)\n"
"v_mfma_f32_16x16x32_bf16 v[100:103], a[68:71], v[56:59], v[100:103]\n"
"ds_write_b64 v241, v[74:75] offset:37376\n"
"v_cvt_pk_bf16_f32 v76, v80, v81\n"
"buffer_load_dwordx4 a[76:79], v238, s[56:59], s96 offen offset:0 sc1\n"
"s_waitcnt lgkmcnt(2)\n"
"v_mfma_f32_16x16x32_bf16 v[104:107], a[72:75], v[56:59], v[104:107]\n"
"v_cvt_pk_bf16_f32 v77, v82, v83\n"
"v_cvt_pk_bf16_f32 v78, v84, v85\n"
"v_mfma_f32_16x16x32_bf16 v[108:111], a[40:43], v[60:63], v[108:111]\n"
"ds_write_b64 v241, v[76:77] offset:37888\n"
"v_cvt_pk_bf16_f32 v79, v86, v87\n"
"v_mfma_f32_16x16x32_bf16 v[112:115], a[44:47], v[60:63], v[112:115]\n"
"ds_write_b64 v241, v[78:79] offset:38400\n"
"v_cvt_pk_bf16_f32 v80, v88, v89\n"
"buffer_load_dwordx4 a[96:99], v238, s[56:59], s97 offen offset:0 sc1\n"
"v_cvt_pk_bf16_f32 v81, v90, v91\n"
"v_mfma_f32_16x16x32_bf16 v[116:119], a[48:51], v[60:63], v[116:119]\n"
"ds_write_b64 v241, v[80:81] offset:38912\n"
"v_cvt_pk_bf16_f32 v82, v92, v93\n"
"v_cvt_pk_bf16_f32 v83, v94, v95\n"
"v_mfma_f32_16x16x32_bf16 v[120:123], a[52:55], v[60:63], v[120:123]\n"
"ds_write_b64 v241, v[82:83] offset:39424\n"
"v_cvt_pk_bf16_f32 v84, v96, v97\n"
"v_cvt_pk_bf16_f32 v85, v98, v99\n"
"v_mfma_f32_16x16x32_bf16 v[124:127], a[56:59], v[60:63], v[124:127]\n"
"ds_write_b64 v241, v[84:85] offset:39936\n"
"v_cvt_pk_bf16_f32 v86, v100, v101\n"
"buffer_load_dwordx4 a[116:119], v238, s[56:59], s98 offen offset:0 sc1\n"
"v_cvt_pk_bf16_f32 v87, v102, v103\n"
"v_mfma_f32_16x16x32_bf16 v[128:131], a[60:63], v[60:63], v[128:131]\n"
"ds_write_b64 v241, v[86:87] offset:40448\n"
"v_cvt_pk_bf16_f32 v88, v104, v105\n"
"v_cvt_pk_bf16_f32 v89, v106, v107\n"
"v_mfma_f32_16x16x32_bf16 v[132:135], a[64:67], v[60:63], v[132:135]\n"
"ds_write_b64 v241, v[88:89] offset:40960\n"
"v_cvt_pk_bf16_f32 v90, v108, v109\n"
"v_cvt_pk_bf16_f32 v91, v110, v111\n"
"buffer_load_dwordx4 a[80:83], v238, s[56:59], s96 offen offset:1024 sc1\n"
"v_mfma_f32_16x16x32_bf16 v[136:139], a[68:71], v[60:63], v[136:139]\n"
"ds_write_b64 v241, v[90:91] offset:41472\n"
"v_cvt_pk_bf16_f32 v92, v112, v113\n"
"v_cvt_pk_bf16_f32 v93, v114, v115\n"
"v_mfma_f32_16x16x32_bf16 v[140:143], a[72:75], v[60:63], v[140:143]\n"
"ds_write_b64 v241, v[92:93] offset:41984\n"
"v_cvt_pk_bf16_f32 v94, v116, v117\n"
"v_cvt_pk_bf16_f32 v95, v118, v119\n"
"ds_write_b64 v241, v[94:95] offset:42496\n"
"buffer_load_dwordx4 a[100:103], v238, s[56:59], s97 offen offset:1024 sc1\n"
"v_cvt_pk_bf16_f32 v96, v120, v121\n"
"v_cvt_pk_bf16_f32 v97, v122, v123\n"
"ds_write_b64 v241, v[96:97] offset:43008\n"
"v_cvt_pk_bf16_f32 v98, v124, v125\n"
"v_cvt_pk_bf16_f32 v99, v126, v127\n"
"ds_write_b64 v241, v[98:99] offset:43520\n"
"v_cvt_pk_bf16_f32 v100, v128, v129\n"
"v_cvt_pk_bf16_f32 v101, v130, v131\n"
"buffer_load_dwordx4 a[120:123], v238, s[56:59], s98 offen offset:1024 sc1\n"
"ds_write_b64 v241, v[100:101] offset:44032\n"
"v_cvt_pk_bf16_f32 v102, v132, v133\n"
"v_cvt_pk_bf16_f32 v103, v134, v135\n"
"ds_write_b64 v241, v[102:103] offset:44544\n"
"v_cvt_pk_bf16_f32 v104, v136, v137\n"
"v_cvt_pk_bf16_f32 v105, v138, v139\n"
"ds_write_b64 v241, v[104:105] offset:45056\n"
"v_cvt_pk_bf16_f32 v106, v140, v141\n"
"buffer_load_dwordx4 a[84:87], v238, s[56:59], s96 offen offset:2048 sc1\n"
"v_cvt_pk_bf16_f32 v107, v142, v143\n"
"ds_write_b64 v241, v[106:107] offset:45568\n"
"v_exp_f32 v42, v42\n"
"v_exp_f32 v43, v43\n"
"v_exp_f32 v44, v44\n"
"v_exp_f32 v45, v45\n"
"buffer_load_dwordx4 a[104:107], v238, s[56:59], s97 offen offset:2048 sc1\n"
"v_exp_f32 v46, v46\n"
"v_exp_f32 v47, v47\n"
"v_exp_f32 v48, v48\n"
"v_exp_f32 v49, v49\n"
"v_lshlrev_b32 v13, 5, v21\n"
"v_cvt_pk_bf16_f32 v64, v40, v41\n"
"v_cvt_pk_bf16_f32 v65, v42, v43\n"
"v_cvt_pk_bf16_f32 v66, v44, v45\n"
"v_cvt_pk_bf16_f32 v67, v46, v47\n"
"v_add_u32 v10, 36864, v242\n"
"v_add_u32 v11, 36864, v246\n"
"v_add_u32 v13, 119808, v13\n"
"s_waitcnt vmcnt(19)\n"
"v_cmp_eq_u32 vcc, s49, v20\n"
"s_nop 1\n"
"s_and_b32 s48, vcc_lo, vcc_hi\n"
"s_bitcmp1_b32 s48, 16\n"
"s_cbranch_scc1 L_pzok_%=\n"
"L_pzsp_%=:\n"
"global_load_dword v20, v19, s[66:67] sc1\n"
"s_waitcnt vmcnt(0)\n"
"v_cmp_eq_u32 vcc, s49, v20\n"
"s_nop 1\n"
"s_and_b32 s48, vcc_lo, vcc_hi\n"
"s_sub_u32 s53, s53, 1\n"
"s_cselect_b32 s50, -1, 0\n"
"s_or_b32 s48, s48, s50\n"
"s_max_i32 s53, s53, 0\n"
"s_bitcmp1_b32 s48, 16\n"
"s_cbranch_scc0 L_pzsp_%=\n"
"L_pzok_%=:\n"
"s_cmp_eq_u32 s77, 3\n"
"s_cbranch_scc0 L_e1_%=\n"
"v_mov_b32 v1, 122128\n"
"s_waitcnt lgkmcnt(0)\n"
"L_cn0_%=:\n"
"ds_read_b32 v2, v1\n"
"s_waitcnt lgkmcnt(0)\n"
"v_readfirstlane_b32 s50, v2\n"
"s_sub_u32 s53, s53, 1\n"
"s_nop 0\n"
"s_cselect_b32 s50, 256, s50\n"
"s_max_i32 s53, s53, 0\n"
"s_cmp_eq_u32 s50, 256\n"
"s_cbranch_scc0 L_cn0_%=\n"
"v_add_u32 v6, 122368, v238\n"
"v_add_u32 v8, 154624, v250\n"
"ds_read_b128 a[0:3], v6 offset:0\n"
"ds_read_b64_tr_b16 a[136:137], v8 offset:0\n"
"ds_read_b64_tr_b16 a[138:139], v8 offset:1024\n"
"s_mov_b32 s55, 0xffff0000\n"
"ds_read_b128 a[20:23], v6 offset:4608\n"
"ds_read_b128 a[4:7], v6 offset:1024\n"
"ds_read_b64_tr_b16 a[140:141], v8 offset:2048\n"
"ds_read_b64_tr_b16 a[142:143], v8 offset:3072\n"
"ds_read_b128 a[24:27], v6 offset:5632\n"
"v_add_u32 v7, 122368, v23\n"
"v_add_u32 v9, 110592, v238\n"
"v_accvgpr_write_b32 a18, 0\n"
"v_accvgpr_write_b32 a19, 0\n"
"v_accvgpr_write_b32 a154, 0\n"
"v_accvgpr_write_b32 a155, 0\n"
"v_accvgpr_write_b32 a38, 0\n"
"v_accvgpr_write_b32 a39, 0\n"
"s_waitcnt vmcnt(9)\n"
"v_lshlrev_b32 v96, 16, v230\n"
"v_and_b32 v97, s55, v230\n"
"v_lshlrev_b32 v98, 16, v231\n"
"v_and_b32 v99, s55, v231\n"
"s_waitcnt vmcnt(8)\n"
"v_lshlrev_b32 v100, 16, v232\n"
"v_and_b32 v101, s55, v232\n"
"v_lshlrev_b32 v102, 16, v233\n"
"v_and_b32 v103, s55, v233\n"
"v_lshlrev_b32 v92, 16, v228\n"
"v_and_b32 v93, s55, v228\n"
"s_waitcnt lgkmcnt(5)\n"
"v_mfma_f32_16x16x32_bf16 v[72:75], a[136:139], a[0:3], 0\n"
"ds_read_b128 a[8:11], v6 offset:2048\n"
"ds_read_b64_tr_b16 a[144:145], v8 offset:4096\n"
"ds_read_b64_tr_b16 a[146:147], v8 offset:5120\n"
"ds_read_b128 a[28:31], v6 offset:6656\n"
"ds_read_b128 a[12:15], v6 offset:3072\n"
"s_waitcnt lgkmcnt(9)\n"
"v_mfma_f32_16x16x32_bf16 v[76:79], a[136:139], a[20:23], 0\n"
"ds_read_b64_tr_b16 a[148:149], v8 offset:6144\n"
"ds_read_b64_tr_b16 a[150:151], v8 offset:7168\n"
"s_waitcnt lgkmcnt(8)\n"
"v_mfma_f32_16x16x32_bf16 v[72:75], a[140:143], a[4:7], v[72:75]\n"
"ds_read_b128 a[32:35], v6 offset:7680\n"
"v_lshlrev_b32 v94, 16, v229\n"
"s_waitcnt lgkmcnt(8)\n"
"v_mfma_f32_16x16x32_bf16 v[76:79], a[140:143], a[24:27], v[76:79]\n"
"ds_read_b64 a[16:17], v7 offset:4096\n"
"ds_read_b64_tr_b16 a[152:153], v8 offset:8192\n"
"ds_read_b128 v[112:115], v13 offset:0\n"
"v_and_b32 v95, s55, v229\n"
"v_add_u32 v1, 114688, v23\n"
"ds_read_b128 v[116:119], v13 offset:16\n"
"ds_read_b64 a[36:37], v7 offset:8704\n"
"ds_read_b128 v[120:123], v13 offset:512\n"
"s_waitcnt lgkmcnt(11)\n"
"v_mfma_f32_16x16x32_bf16 v[72:75], a[144:147], a[8:11], v[72:75]\n"
"ds_read_b128 v[124:127], v13 offset:528\n"
"s_waitcnt lgkmcnt(11)\n"
"v_mfma_f32_16x16x32_bf16 v[76:79], a[144:147], a[28:31], v[76:79]\n"
"ds_read_b128 v[128:131], v13 offset:1024\n"
"s_waitcnt lgkmcnt(9)\n"
"v_mfma_f32_16x16x32_bf16 v[72:75], a[148:151], a[12:15], v[72:75]\n"
"ds_read_b128 v[132:135], v13 offset:1040\n"
"s_waitcnt lgkmcnt(9)\n"
"v_mfma_f32_16x16x32_bf16 v[76:79], a[148:151], a[32:35], v[76:79]\n"
"ds_read_b128 v[80:83], v9 offset:0\n"
"s_waitcnt lgkmcnt(8)\n"
"v_mfma_f32_16x16x32_bf16 v[72:75], a[152:155], a[16:19], v[72:75]\n"
"ds_read_b128 v[84:87], v9 offset:1024\n"
"ds_read_b128 v[88:91], v9 offset:2048\n"
"s_waitcnt lgkmcnt(7)\n"
"v_mfma_f32_16x16x32_bf16 v[76:79], a[152:155], a[36:39], v[76:79]\n"
"v_pk_add_f32 v[112:113], v[112:113], v[114:115]\n"
"v_pk_add_f32 v[116:117], v[116:117], v[118:119]\n"
"v_pk_add_f32 v[112:113], v[112:113], v[116:117]\n"
"s_waitcnt lgkmcnt(6)\n"
"v_pk_add_f32 v[120:121], v[120:121], v[122:123]\n"
"s_waitcnt lgkmcnt(5)\n"
"v_pk_add_f32 v[124:125], v[124:125], v[126:127]\n"
"v_mul_f32 v112, 0x3be38e39, v112\n"
"v_mul_f32 v113, 0x3be38e39, v113\n"
"v_pk_add_f32 v[120:121], v[120:121], v[124:125]\n"
"v_fma_f32 v113, -v112, v112, v113\n"
"v_mul_f32 v120, 0x3be38e39, v120\n"
"v_mul_f32 v121, 0x3be38e39, v121\n"
"v_max_f32 v113, 0, v113\n"
"v_fma_f32 v121, -v120, v120, v121\n"
"v_add_f32 v113, 0x3727c5ac, v113\n"
"v_max_f32 v121, 0, v121\n"
"s_waitcnt lgkmcnt(4)\n"
"v_pk_add_f32 v[128:129], v[128:129], v[130:131]\n"
"s_waitcnt lgkmcnt(3)\n"
"v_pk_add_f32 v[132:133], v[132:133], v[134:135]\n"
"v_rsq_f32 v137, v113\n"
"v_add_f32 v121, 0x3727c5ac, v121\n"
"v_pk_add_f32 v[128:129], v[128:129], v[132:133]\n"
"v_rsq_f32 v139, v121\n"
"v_mul_f32 v128, 0x3be38e39, v128\n"
"v_mul_f32 v129, 0x3be38e39, v129\n"
"v_pk_mul_f32 v[104:105], v[72:73], v[72:73]\n"
"v_pk_mul_f32 v[106:107], v[74:75], v[74:75]\n"
"v_fma_f32 v129, -v128, v128, v129\n"
"v_pk_fma_f32 v[104:105], v[104:105], s[92:93], v[236:237] op_sel_hi:[1,1,0]\n"
"v_pk_fma_f32 v[106:107], v[106:107], s[92:93], v[236:237] op_sel_hi:[1,1,0]\n"
"v_mul_f32 v136, -v112, v137\n"
"v_max_f32 v129, 0, v129\n"
"v_pk_mul_f32 v[104:105], v[104:105], v[72:73]\n"
"v_pk_mul_f32 v[106:107], v[106:107], v[74:75]\n"
"v_mul_f32 v138, -v120, v139\n"
"v_add_f32 v129, 0x3727c5ac, v129\n"
"s_waitcnt lgkmcnt(2)\n"
"v_pk_fma_f32 v[80:81], v[80:81], v[136:137], v[136:137] op_sel:[0,1,0] op_sel_hi:[1,1,0]\n"
"v_pk_fma_f32 v[82:83], v[82:83], v[136:137], v[136:137] op_sel:[0,1,0] op_sel_hi:[1,1,0]\n"
"v_exp_f32 v104, v104\n"
"v_exp_f32 v105, v105\n"
"v_exp_f32 v106, v106\n"
"v_exp_f32 v107, v107\n"
"v_rsq_f32 v141, v129\n"
"s_waitcnt lgkmcnt(1)\n"
"v_pk_fma_f32 v[84:85], v[84:85], v[138:139], v[138:139] op_sel:[0,1,0] op_sel_hi:[1,1,0]\n"
"v_pk_fma_f32 v[86:87], v[86:87], v[138:139], v[138:139] op_sel:[0,1,0] op_sel_hi:[1,1,0]\n"
"v_pk_mul_f32 v[24:25], v[80:81], v[96:97]\n"
"v_pk_mul_f32 v[26:27], v[82:83], v[98:99]\n"
"v_pk_mul_f32 v[108:109], v[76:77], v[76:77]\n"
"v_pk_mul_f32 v[110:111], v[78:79], v[78:79]\n"
"v_pk_fma_f32 v[24:25], v[84:85], v[100:101], v[24:25]\n"
"v_pk_fma_f32 v[26:27], v[86:87], v[102:103], v[26:27]\n"
"v_pk_fma_f32 v[108:109], v[108:109], s[92:93], v[236:237] op_sel_hi:[1,1,0]\n"
"v_pk_fma_f32 v[110:111], v[110:111], s[92:93], v[236:237] op_sel_hi:[1,1,0]\n"
"v_pk_mul_f32 v[108:109], v[108:109], v[76:77]\n"
"v_pk_mul_f32 v[110:111], v[110:111], v[78:79]\n"
"v_pk_add_f32 v[104:105], v[104:105], v[234:235] op_sel_hi:[1,0]\n"
"v_pk_add_f32 v[106:107], v[106:107], v[234:235] op_sel_hi:[1,0]\n"
"v_mul_f32 v140, -v128, v141\n"
"v_exp_f32 v108, v108\n"
"v_exp_f32 v109, v109\n"
"v_exp_f32 v110, v110\n"
"v_exp_f32 v111, v111\n"
"v_rcp_f32 v104, v104\n"
"v_rcp_f32 v105, v105\n"
"v_rcp_f32 v106, v106\n"
"v_rcp_f32 v107, v107\n"
"s_waitcnt lgkmcnt(0)\n"
"v_pk_fma_f32 v[88:89], v[88:89], v[140:141], v[140:141] op_sel:[0,1,0] op_sel_hi:[1,1,0]\n"
"v_pk_fma_f32 v[90:91], v[90:91], v[140:141], v[140:141] op_sel:[0,1,0] op_sel_hi:[1,1,0]\n"
"v_pk_mul_f32 v[28:29], v[84:85], v[96:97]\n"
"v_pk_mul_f32 v[30:31], v[86:87], v[98:99]\n"
"v_fmac_f32_dpp v24, v84, v92 row_shr:1 row_mask:0xf bank_mask:0xf\n"
"v_fmac_f32_dpp v25, v85, v93 row_shr:1 row_mask:0xf bank_mask:0xf\n"
"v_fmac_f32_dpp v26, v86, v94 row_shr:1 row_mask:0xf bank_mask:0xf\n"
"v_fmac_f32_dpp v27, v87, v95 row_shr:1 row_mask:0xf bank_mask:0xf\n"
"v_pk_fma_f32 v[28:29], v[80:81], v[92:93], v[28:29]\n"
"v_pk_fma_f32 v[30:31], v[82:83], v[94:95], v[30:31]\n"
"v_pk_add_f32 v[108:109], v[108:109], v[234:235] op_sel_hi:[1,0]\n"
"v_pk_add_f32 v[110:111], v[110:111], v[234:235] op_sel_hi:[1,0]\n"
"v_pk_mul_f32 v[72:73], v[72:73], v[104:105]\n"
"v_pk_mul_f32 v[74:75], v[74:75], v[106:107]\n"
"v_fmac_f32_dpp v24, v88, v92 row_shl:15 row_mask:0xf bank_mask:0xf\n"
"v_fmac_f32_dpp v25, v89, v93 row_shl:15 row_mask:0xf bank_mask:0xf\n"
"v_fmac_f32_dpp v26, v90, v94 row_shl:15 row_mask:0xf bank_mask:0xf\n"
"v_fmac_f32_dpp v27, v91, v95 row_shl:15 row_mask:0xf bank_mask:0xf\n"
"v_rcp_f32 v108, v108\n"
"v_rcp_f32 v109, v109\n"
"v_rcp_f32 v110, v110\n"
"v_rcp_f32 v111, v111\n"
"v_fmac_f32_dpp v28, v80, v100 row_shl:1 row_mask:0xf bank_mask:0xf\n"
"v_fmac_f32_dpp v29, v81, v101 row_shl:1 row_mask:0xf bank_mask:0xf\n"
"v_fmac_f32_dpp v30, v82, v102 row_shl:1 row_mask:0xf bank_mask:0xf\n"
"v_fmac_f32_dpp v31, v83, v103 row_shl:1 row_mask:0xf bank_mask:0xf\n"
"v_pk_mul_f32 v[24:25], v[24:25], v[72:73]\n"
"v_pk_mul_f32 v[26:27], v[26:27], v[74:75]\n"
"v_cvt_pk_bf16_f32 v2, v24, v25\n"
"v_cvt_pk_bf16_f32 v3, v26, v27\n"
"ds_write_b64 v1, v[2:3] offset:0\n"
"v_pk_mul_f32 v[76:77], v[76:77], v[108:109]\n"
"v_pk_mul_f32 v[78:79], v[78:79], v[110:111]\n"
"v_fmac_f32_dpp v28, v88, v100 row_shr:15 row_mask:0xf bank_mask:0xf\n"
"v_fmac_f32_dpp v29, v89, v101 row_shr:15 row_mask:0xf bank_mask:0xf\n"
"v_fmac_f32_dpp v30, v90, v102 row_shr:15 row_mask:0xf bank_mask:0xf\n"
"v_fmac_f32_dpp v31, v91, v103 row_shr:15 row_mask:0xf bank_mask:0xf\n"
"v_pk_mul_f32 v[28:29], v[28:29], v[76:77]\n"
"v_pk_mul_f32 v[30:31], v[30:31], v[78:79]\n"
"v_cvt_pk_bf16_f32 v4, v28, v29\n"
"v_cvt_pk_bf16_f32 v5, v30, v31\n"
"ds_write_b64 v1, v[4:5] offset:4608\n"
"L_e1_%=:\n"
"s_waitcnt lgkmcnt(0)\n"
"s_barrier\n"
"ds_read_b64 v[72:73], v10 offset:0\n"
"ds_read_b64 v[74:75], v10 offset:9216\n"
"ds_read_b64 v[80:81], v10 offset:512\n"
"ds_read_b64 v[82:83], v10 offset:9728\n"
"ds_read_b64 v[88:89], v10 offset:4608\n"
"ds_read_b64 v[90:91], v10 offset:13824\n"
"ds_read_b64 v[96:97], v10 offset:5120\n"
"ds_read_b64 v[98:99], v10 offset:14336\n"
"ds_read_b64 v[104:105], v11 offset:0\n"
"ds_read_b64 v[106:107], v11 offset:9216\n"
"ds_read_b64 v[76:77], v10 offset:18432\n"
"buffer_load_dwordx4 v[24:27], v254, s[36:39], s83 offen offset:0\n"
"ds_read_b64 v[78:79], v10 offset:27648\n"
"ds_read_b64 v[84:85], v10 offset:18944\n"
"ds_read_b64 v[86:87], v10 offset:28160\n"
"buffer_load_dwordx4 v[28:31], v254, s[36:39], s83 offen offset:288\n"
"ds_read_b64 v[92:93], v10 offset:23040\n"
"ds_read_b64 v[94:95], v10 offset:32256\n"
"ds_read_b64 v[100:101], v10 offset:23552\n"
"v_mfma_f32_16x16x32_bf16 v[164:167], a[60:63], v[64:67], v[164:167]\n"
"s_add_u32 s50, s45, 73728\n"
"s_sub_u32 s80, s70, 0x2000\n"
"s_subb_u32 s81, s71, 0\n"
"ds_read_b64 v[102:103], v10 offset:32768\n"
"ds_read_b64 v[108:109], v11 offset:18432\n"
"ds_read_b64 v[110:111], v11 offset:27648\n"
"s_waitcnt lgkmcnt(15)\n"
"v_mfma_f32_16x16x32_bf16 v[112:115], a[202:205], v[72:75], 0\n"
"buffer_load_dwordx2 v[32:33], v255, s[36:39], s84 offen offset:256\n"
"s_waitcnt lgkmcnt(15)\n"
"v_mfma_f32_16x16x32_bf16 v[116:119], a[202:205], v[80:83], 0\n"
"s_mov_b32 s55, 0xffff0000\n"
"ds_read_b128 v[44:47], v13 offset:0\n"
"ds_read_b128 v[60:63], v13 offset:1024\n"
"s_waitcnt lgkmcnt(15)\n"
"v_mfma_f32_16x16x32_bf16 v[120:123], a[202:205], v[88:91], 0\n"
"ds_read_b128 v[56:59], v13 offset:528\n"
"v_add_u32 v8, s50, v250\n"
"s_waitcnt lgkmcnt(15)\n"
"v_mfma_f32_16x16x32_bf16 v[124:127], a[202:205], v[96:99], 0\n"
"ds_read_b64_tr_b16 a[242:243], v8 offset:8192\n"
"v_add_u32 v9, s50, v251\n"
"s_waitcnt lgkmcnt(14)\n"
"v_mfma_f32_16x16x32_bf16 v[128:131], a[202:205], v[104:107], 0\n"
"s_lshl_b32 s50, s77, 10\n"
"s_add_u32 s50, s50, 110592\n"
"v_add_u32 v12, s50, v238\n"
"s_waitcnt lgkmcnt(12)\n"
"v_mfma_f32_16x16x32_bf16 v[112:115], a[202:205], v[76:79], v[112:115]\n"
"s_and_b32 s50, s77, 2\n"
"s_mul_i32 s50, s50, 2560\n"
"v_subrev_u32 v252, s50, v23\n"
"s_waitcnt lgkmcnt(10)\n"
"v_mfma_f32_16x16x32_bf16 v[116:119], a[202:205], v[84:87], v[116:119]\n"
"s_and_b32 s50, s77, 1\n"
"s_mul_i32 s50, s50, 4608\n"
"v_add_u32 v252, 0x2000, v252\n"
"s_waitcnt lgkmcnt(8)\n"
"v_mfma_f32_16x16x32_bf16 v[120:123], a[202:205], v[92:95], v[120:123]\n"
"s_waitcnt lgkmcnt(6)\n"
"v_cvt_pk_bf16_f32 v132, v112, v113\n"
"v_mfma_f32_16x16x32_bf16 v[124:127], a[202:205], v[100:103], v[124:127]\n"
"s_waitcnt lgkmcnt(4)\n"
"v_cvt_pk_bf16_f32 v133, v114, v115\n"
"v_cvt_pk_bf16_f32 v134, v116, v117\n"
"v_mfma_f32_16x16x32_bf16 v[128:131], a[202:205], v[108:111], v[128:131]\n"
"v_cvt_pk_bf16_f32 v135, v118, v119\n"
"v_cvt_pk_bf16_f32 v136, v120, v121\n"
"v_cvt_pk_bf16_f32 v137, v122, v123\n"
"global_store_dwordx4 v238, v[132:135], s[68:69] sc1\n"
"v_cvt_pk_bf16_f32 v138, v124, v125\n"
"v_cvt_pk_bf16_f32 v139, v126, v127\n"
"global_store_dwordx4 v238, v[136:139], s[68:69] offset:1024 sc1\n"
"s_nop 0\n"
"v_cvt_pk_bf16_f32 v140, v128, v129\n"
"v_cvt_pk_bf16_f32 v141, v130, v131\n"
"v_mfma_f32_16x16x32_bf16 v[168:171], a[64:67], v[64:67], v[168:171]\n"
"v_exp_f32 v50, v50\n"
"v_mfma_f32_16x16x32_bf16 v[172:175], a[68:71], v[64:67], v[172:175]\n"
"v_exp_f32 v51, v51\n"
"global_store_dwordx2 v23, v[140:141], s[68:69] offset:2048 sc1\n"
"v_mfma_f32_16x16x32_bf16 v[144:147], a[40:43], v[64:67], v[144:147]\n"
"v_exp_f32 v52, v52\n"
"v_mfma_f32_16x16x32_bf16 v[176:179], a[72:75], v[64:67], v[176:179]\n"
"buffer_load_dwordx4 a[124:127], v238, s[56:59], s98 offen offset:2048 sc1\n"
"v_mfma_f32_16x16x32_bf16 v[148:151], a[44:47], v[64:67], v[148:151]\n"
"v_exp_f32 v53, v53\n"
"v_exp_f32 v54, v54\n"
"v_exp_f32 v55, v55\n"
"v_mfma_f32_16x16x32_bf16 v[152:155], a[48:51], v[64:67], v[152:155]\n"
"v_cvt_pk_bf16_f32 v68, v48, v49\n"
"v_cvt_pk_bf16_f32 v69, v50, v51\n"
"v_cvt_pk_bf16_f32 v70, v52, v53\n"
"buffer_load_dwordx4 a[88:91], v238, s[56:59], s96 offen offset:3072 sc1\n"
"v_cvt_pk_bf16_f32 v71, v54, v55\n"
"v_mfma_f32_16x16x32_bf16 v[160:163], a[56:59], v[64:67], v[160:163]\n"
"ds_read_b128 v[48:51], v13 offset:16\n"
"ds_read_b128 v[52:55], v13 offset:512\n"
"v_mfma_f32_16x16x32_bf16 v[200:203], a[60:63], v[68:71], v[200:203]\n"
"ds_read_b64_tr_b16 a[60:61], v8 offset:0\n"
"ds_read_b64_tr_b16 a[62:63], v8 offset:1024\n"
"v_mfma_f32_16x16x32_bf16 v[204:207], a[64:67], v[68:71], v[204:207]\n"
"ds_read_b64_tr_b16 a[64:65], v8 offset:2048\n"
"ds_read_b64_tr_b16 a[66:67], v8 offset:3072\n"
"buffer_load_dwordx4 a[108:111], v238, s[56:59], s97 offen offset:3072 sc1\n"
"v_mfma_f32_16x16x32_bf16 v[208:211], a[68:71], v[68:71], v[208:211]\n"
"ds_read_b64_tr_b16 a[68:69], v8 offset:4096\n"
"ds_read_b64_tr_b16 a[70:71], v8 offset:5120\n"
"v_mfma_f32_16x16x32_bf16 v[212:215], a[72:75], v[68:71], v[212:215]\n"
"ds_read_b64_tr_b16 a[72:73], v8 offset:6144\n"
"ds_read_b64_tr_b16 a[74:75], v8 offset:7168\n"
"v_add_u32 v6, 122368, v238\n"
"v_mfma_f32_16x16x32_bf16 v[180:183], a[40:43], v[68:71], v[180:183]\n"
"ds_read_b128 a[0:3], v6 offset:0\n"
"ds_read_b128 a[20:23], v6 offset:4608\n"
"buffer_load_dwordx4 a[128:131], v238, s[56:59], s98 offen offset:3072 sc1\n"
"v_mfma_f32_16x16x32_bf16 v[184:187], a[44:47], v[68:71], v[184:187]\n"
"ds_read_b128 a[4:7], v6 offset:1024\n"
"ds_read_b128 a[24:27], v6 offset:5632\n"
"v_mfma_f32_16x16x32_bf16 v[156:159], a[52:55], v[64:67], v[156:159]\n"
"ds_read_b128 a[8:11], v6 offset:2048\n"
"ds_read_b128 a[28:31], v6 offset:6656\n"
"v_add_u32 v7, 122368, v23\n"
"v_cvt_pk_bf16_f32 v144, v144, v145\n"
"v_cvt_pk_bf16_f32 v145, v146, v147\n"
"v_cvt_pk_bf16_f32 v146, v148, v149\n"
"v_cvt_pk_bf16_f32 v148, v152, v153\n"
"buffer_load_dwordx4 a[92:95], v237, s[56:59], s96 offen offset:0 sc1\n"
"ds_read_b128 a[12:15], v6 offset:3072\n"
"ds_read_b128 a[32:35], v6 offset:7680\n"
"ds_read_b64 a[16:17], v7 offset:4096\n"
"ds_read_b64 a[36:37], v7 offset:8704\n"
"v_cvt_pk_bf16_f32 v149, v154, v155\n"
"v_cvt_pk_bf16_f32 v153, v162, v163\n"
"v_cvt_pk_bf16_f32 v154, v164, v165\n"
"v_cvt_pk_bf16_f32 v162, v180, v181\n"
"v_cvt_pk_bf16_f32 v163, v182, v183\n"
"s_waitcnt lgkmcnt(9)\n"
"v_mfma_f32_16x16x32_bf16 v[180:183], a[60:63], a[0:3], 0\n"
"ds_write_b64 v241, v[144:145] offset:0\n"
"ds_read_b128 v[64:67], v13 offset:1040\n"
"buffer_load_dwordx4 a[112:115], v237, s[56:59], s97 offen offset:0 sc1\n"
"s_waitcnt lgkmcnt(9)\n"
"v_mfma_f32_16x16x32_bf16 v[180:183], a[64:67], a[4:7], v[180:183]\n"
"v_cvt_pk_bf16_f32 v164, v184, v185\n"
"v_cvt_pk_bf16_f32 v165, v186, v187\n"
"v_mfma_f32_16x16x32_bf16 v[184:187], a[60:63], a[20:23], 0\n"
"ds_read_b64_tr_b16 a[60:61], v9 offset:0\n"
"ds_read_b64_tr_b16 a[62:63], v9 offset:1024\n"
"s_waitcnt lgkmcnt(10)\n"
"v_mfma_f32_16x16x32_bf16 v[184:187], a[64:67], a[24:27], v[184:187]\n"
"ds_read_b64_tr_b16 a[64:65], v9 offset:2048\n"
"ds_read_b64_tr_b16 a[66:67], v9 offset:3072\n"
"s_waitcnt lgkmcnt(11)\n"
"v_mfma_f32_16x16x32_bf16 v[180:183], a[68:71], a[8:11], v[180:183]\n"
"buffer_load_dwordx4 a[132:135], v237, s[56:59], s98 offen offset:0 sc1\n"
"s_waitcnt lgkmcnt(10)\n"
"v_mfma_f32_16x16x32_bf16 v[184:187], a[68:71], a[28:31], v[184:187]\n"
"ds_read_b64_tr_b16 a[68:69], v9 offset:4096\n"
"ds_read_b64_tr_b16 a[70:71], v9 offset:5120\n"
"s_waitcnt lgkmcnt(11)\n"
"v_mfma_f32_16x16x32_bf16 v[180:183], a[72:75], a[12:15], v[180:183]\n"
"s_waitcnt lgkmcnt(10)\n"
"v_cvt_pk_bf16_f32 v147, v150, v151\n"
"v_cvt_pk_bf16_f32 v150, v156, v157\n"
"v_mfma_f32_16x16x32_bf16 v[184:187], a[72:75], a[32:35], v[184:187]\n"
"ds_write_b64 v241, v[146:147] offset:512\n"
"ds_read_b64_tr_b16 a[72:73], v9 offset:6144\n"
"v_mfma_f32_16x16x32_bf16 v[188:191], a[48:51], v[68:71], v[188:191]\n"
"buffer_load_dwordx4 a[136:139], v238, s[56:59], s86 offen offset:0 sc1\n"
"ds_write_b64 v241, v[148:149] offset:1024\n"
"v_cvt_pk_bf16_f32 v151, v158, v159\n"
"v_mfma_f32_16x16x32_bf16 v[192:195], a[52:55], v[68:71], v[192:195]\n"
"ds_write_b64 v241, v[150:151] offset:1536\n"
"ds_read_b64_tr_b16 a[74:75], v9 offset:7168\n"
"v_cvt_pk_bf16_f32 v152, v160, v161\n"
"v_accvgpr_write_b32 a18, 0\n"
"v_accvgpr_write_b32 a19, 0\n"
"v_accvgpr_write_b32 a38, 0\n"
"s_waitcnt lgkmcnt(14)\n"
"v_mfma_f32_16x16x32_bf16 v[180:183], a[242:245], a[16:19], v[180:183]\n"
"ds_write_b64 v241, v[152:153] offset:2048\n"
"v_accvgpr_write_b32 a39, 0\n"
"buffer_load_dwordx4 a[140:143], v238, s[56:59], s86 offen offset:1024 sc1\n"
"s_waitcnt lgkmcnt(14)\n"
"v_mfma_f32_16x16x32_bf16 v[184:187], a[242:245], a[36:39], v[184:187]\n"
"ds_read_b64_tr_b16 a[242:243], v9 offset:8192\n"
"v_cvt_pk_bf16_f32 v155, v166, v167\n"
"v_cvt_pk_bf16_f32 v156, v168, v169\n"
"v_cvt_pk_bf16_f32 v157, v170, v171\n"
"v_mfma_f32_16x16x32_bf16 v[196:199], a[56:59], v[68:71], v[196:199]\n"
"ds_write_b64 v241, v[154:155] offset:2560\n"
"v_cvt_pk_bf16_f32 v158, v172, v173\n"
"buffer_load_dwordx4 a[144:147], v238, s[56:59], s86 offen offset:2048 sc1\n"
"ds_write_b64 v241, v[156:157] offset:3072\n"
"v_cvt_pk_bf16_f32 v159, v174, v175\n"
"ds_write_b64 v241, v[158:159] offset:3584\n"
"v_cvt_pk_bf16_f32 v160, v176, v177\n"
"v_cvt_pk_bf16_f32 v161, v178, v179\n"
"v_cvt_pk_bf16_f32 v166, v188, v189\n"
"v_cvt_pk_bf16_f32 v167, v190, v191\n"
"s_waitcnt lgkmcnt(14)\n"
"v_mfma_f32_16x16x32_bf16 v[188:191], a[60:63], a[0:3], 0\n"
"ds_write_b64 v241, v[160:161] offset:4096\n"
"v_cvt_pk_bf16_f32 v168, v192, v193\n"
"buffer_load_dwordx4 a[148:151], v238, s[56:59], s86 offen offset:3072 sc1\n"
"s_waitcnt lgkmcnt(13)\n"
"v_mfma_f32_16x16x32_bf16 v[188:191], a[64:67], a[4:7], v[188:191]\n"
"ds_write_b64 v241, v[162:163] offset:4608\n"
"v_cvt_pk_bf16_f32 v169, v194, v195\n"
"s_waitcnt lgkmcnt(12)\n"
"v_mfma_f32_16x16x32_bf16 v[188:191], a[68:71], a[8:11], v[188:191]\n"
"ds_write_b64 v241, v[164:165] offset:5120\n"
"v_cvt_pk_bf16_f32 v170, v196, v197\n"
"v_mfma_f32_16x16x32_bf16 v[192:195], a[60:63], a[20:23], 0\n"
"ds_write_b64 v241, v[166:167] offset:5632\n"
"v_cvt_pk_bf16_f32 v171, v198, v199\n"
"s_waitcnt lgkmcnt(9)\n"
"v_mfma_f32_16x16x32_bf16 v[188:191], a[72:75], a[12:15], v[188:191]\n"
"buffer_load_dwordx4 a[152:155], v237, s[56:59], s86 offen offset:0 sc1\n"
"v_mfma_f32_16x16x32_bf16 v[192:195], a[64:67], a[24:27], v[192:195]\n"
"ds_write_b64 v241, v[168:169] offset:6144\n"
"v_cvt_pk_bf16_f32 v172, v200, v201\n"
"s_waitcnt lgkmcnt(8)\n"
"v_mfma_f32_16x16x32_bf16 v[188:191], a[242:245], a[16:19], v[188:191]\n"
"ds_write_b64 v241, v[170:171] offset:6656\n"
"v_cvt_pk_bf16_f32 v173, v202, v203\n"
"v_mfma_f32_16x16x32_bf16 v[192:195], a[68:71], a[28:31], v[192:195]\n"
"ds_write_b64 v241, v[172:173] offset:7168\n"
"v_cvt_pk_bf16_f32 v174, v204, v205\n"
"v_mfma_f32_16x16x32_bf16 v[192:195], a[72:75], a[32:35], v[192:195]\n"
"s_waitcnt vmcnt(30)\n"
"v_permlane16_swap_b32 v220, v222\n"
"v_mfma_f32_16x16x32_bf16 v[192:195], a[242:245], a[36:39], v[192:195]\n"
"v_permlane16_swap_b32 v221, v223\n"
"v_cvt_pk_bf16_f32 v175, v206, v207\n"
"s_waitcnt vmcnt(29)\n"
"v_permlane16_swap_b32 v224, v226\n"
"v_pk_add_f32 v[44:45], v[44:45], v[46:47]\n"
"v_pk_add_f32 v[48:49], v[48:49], v[50:51]\n"
"v_pk_add_f32 v[44:45], v[44:45], v[48:49]\n"
"v_pk_add_f32 v[60:61], v[60:61], v[62:63]\n"
"v_pk_add_f32 v[64:65], v[64:65], v[66:67]\n"
"v_mul_f32 v44, 0x3be38e39, v44\n"
"v_mul_f32 v45, 0x3be38e39, v45\n"
"v_pk_add_f32 v[52:53], v[52:53], v[54:55]\n"
"v_pk_add_f32 v[56:57], v[56:57], v[58:59]\n"
"v_pk_add_f32 v[60:61], v[60:61], v[64:65]\n"
"v_fma_f32 v45, -v44, v44, v45\n"
"v_pk_add_f32 v[52:53], v[52:53], v[56:57]\n"
"v_mul_f32 v60, 0x3be38e39, v60\n"
"v_mul_f32 v61, 0x3be38e39, v61\n"
"v_max_f32 v45, 0, v45\n"
"v_mul_f32 v52, 0x3be38e39, v52\n"
"v_mul_f32 v53, 0x3be38e39, v53\n"
"v_fma_f32 v61, -v60, v60, v61\n"
"v_add_f32 v45, 0x3727c5ac, v45\n"
"v_fma_f32 v53, -v52, v52, v53\n"
"v_max_f32 v61, 0, v61\n"
"v_rsq_f32 v69, v45\n"
"v_max_f32 v53, 0, v53\n"
"v_add_f32 v61, 0x3727c5ac, v61\n"
"v_add_f32 v53, 0x3727c5ac, v53\n"
"v_rsq_f32 v143, v61\n"
"v_rsq_f32 v71, v53\n"
"v_mul_f32 v68, -v44, v69\n"
"v_pk_mul_f32 v[72:73], v[180:181], v[180:181]\n"
"v_pk_mul_f32 v[74:75], v[182:183], v[182:183]\n"
"v_pk_mul_f32 v[80:81], v[188:189], v[188:189]\n"
"v_pk_mul_f32 v[82:83], v[190:191], v[190:191]\n"
"v_permlane16_swap_b32 v225, v227\n"
"v_mul_f32 v142, -v60, v143\n"
"v_accvgpr_read_b32 v44, a206\n"
"v_accvgpr_read_b32 v45, a207\n"
"v_accvgpr_read_b32 v46, a208\n"
"v_accvgpr_read_b32 v47, a209\n"
"v_accvgpr_read_b32 v56, a218\n"
"v_accvgpr_read_b32 v57, a219\n"
"v_accvgpr_read_b32 v58, a220\n"
"v_accvgpr_read_b32 v59, a221\n"
"v_pk_fma_f32 v[72:73], v[72:73], s[92:93], v[236:237] op_sel_hi:[1,1,0]\n"
"v_pk_fma_f32 v[74:75], v[74:75], s[92:93], v[236:237] op_sel_hi:[1,1,0]\n"
"v_pk_fma_f32 v[80:81], v[80:81], s[92:93], v[236:237] op_sel_hi:[1,1,0]\n"
"v_pk_fma_f32 v[82:83], v[82:83], s[92:93], v[236:237] op_sel_hi:[1,1,0]\n"
"v_permlane16_swap_b32 v216, v218\n"
"v_permlane16_swap_b32 v217, v219\n"
"v_mul_f32 v70, -v52, v71\n"
"v_accvgpr_read_b32 v48, a210\n"
"v_accvgpr_read_b32 v49, a211\n"
"v_accvgpr_read_b32 v50, a212\n"
"v_accvgpr_read_b32 v51, a213\n"
"v_accvgpr_read_b32 v60, a222\n"
"v_accvgpr_read_b32 v61, a223\n"
"v_accvgpr_read_b32 v62, a224\n"
"v_accvgpr_read_b32 v63, a225\n"
"v_pk_mul_f32 v[72:73], v[72:73], v[180:181]\n"
"v_pk_mul_f32 v[74:75], v[74:75], v[182:183]\n"
"v_pk_mul_f32 v[80:81], v[80:81], v[188:189]\n"
"v_pk_mul_f32 v[82:83], v[82:83], v[190:191]\n"
"v_lshlrev_b32 v152, 16, v220\n"
"v_and_b32 v153, s55, v220\n"
"v_lshlrev_b32 v154, 16, v221\n"
"v_and_b32 v155, s55, v221\n"
"v_lshlrev_b32 v156, 16, v222\n"
"v_and_b32 v157, s55, v222\n"
"v_lshlrev_b32 v158, 16, v223\n"
"v_and_b32 v159, s55, v223\n"
"v_pk_fma_f32 v[44:45], v[44:45], v[68:69], v[68:69] op_sel:[0,1,0] op_sel_hi:[1,1,0]\n"
"v_pk_fma_f32 v[46:47], v[46:47], v[68:69], v[68:69] op_sel:[0,1,0] op_sel_hi:[1,1,0]\n"
"v_pk_fma_f32 v[56:57], v[56:57], v[68:69], v[68:69] op_sel:[0,1,0] op_sel_hi:[1,1,0]\n"
"v_pk_fma_f32 v[58:59], v[58:59], v[68:69], v[68:69] op_sel:[0,1,0] op_sel_hi:[1,1,0]\n"
"v_exp_f32 v72, v72\n"
"v_exp_f32 v73, v73\n"
"v_exp_f32 v74, v74\n"
"v_exp_f32 v75, v75\n"
"v_exp_f32 v80, v80\n"
"v_exp_f32 v81, v81\n"
"v_exp_f32 v82, v82\n"
"v_exp_f32 v83, v83\n"
"v_lshlrev_b32 v160, 16, v224\n"
"v_and_b32 v161, s55, v224\n"
"v_lshlrev_b32 v162, 16, v225\n"
"v_and_b32 v163, s55, v225\n"
"v_lshlrev_b32 v164, 16, v226\n"
"v_and_b32 v165, s55, v226\n"
"v_lshlrev_b32 v166, 16, v227\n"
"v_and_b32 v167, s55, v227\n"
"v_pk_fma_f32 v[48:49], v[48:49], v[70:71], v[70:71] op_sel:[0,1,0] op_sel_hi:[1,1,0]\n"
"v_pk_fma_f32 v[50:51], v[50:51], v[70:71], v[70:71] op_sel:[0,1,0] op_sel_hi:[1,1,0]\n"
"v_pk_mul_f32 v[112:113], v[44:45], v[152:153]\n"
"v_pk_mul_f32 v[114:115], v[46:47], v[154:155]\n"
"v_pk_fma_f32 v[60:61], v[60:61], v[70:71], v[70:71] op_sel:[0,1,0] op_sel_hi:[1,1,0]\n"
"v_pk_fma_f32 v[62:63], v[62:63], v[70:71], v[70:71] op_sel:[0,1,0] op_sel_hi:[1,1,0]\n"
"v_pk_mul_f32 v[120:121], v[56:57], v[156:157]\n"
"v_pk_mul_f32 v[122:123], v[58:59], v[158:159]\n"
"v_cvt_pk_bf16_f32 v176, v208, v209\n"
"v_cvt_pk_bf16_f32 v177, v210, v211\n"
"v_lshlrev_b32 v144, 16, v216\n"
"v_and_b32 v145, s55, v216\n"
"v_lshlrev_b32 v146, 16, v217\n"
"v_and_b32 v147, s55, v217\n"
"v_lshlrev_b32 v148, 16, v218\n"
"v_and_b32 v149, s55, v218\n"
"v_lshlrev_b32 v150, 16, v219\n"
"v_and_b32 v151, s55, v219\n"
"v_pk_fma_f32 v[112:113], v[48:49], v[160:161], v[112:113]\n"
"v_pk_fma_f32 v[114:115], v[50:51], v[162:163], v[114:115]\n"
"v_pk_fma_f32 v[120:121], v[60:61], v[164:165], v[120:121]\n"
"v_pk_fma_f32 v[122:123], v[62:63], v[166:167], v[122:123]\n"
"v_accvgpr_read_b32 v52, a214\n"
"v_accvgpr_read_b32 v53, a215\n"
"v_accvgpr_read_b32 v54, a216\n"
"v_accvgpr_read_b32 v55, a217\n"
"v_accvgpr_read_b32 v64, a226\n"
"v_accvgpr_read_b32 v65, a227\n"
"v_accvgpr_read_b32 v66, a228\n"
"v_accvgpr_read_b32 v67, a229\n"
"v_pk_add_f32 v[72:73], v[72:73], v[234:235] op_sel_hi:[1,0]\n"
"v_pk_add_f32 v[74:75], v[74:75], v[234:235] op_sel_hi:[1,0]\n"
"v_pk_add_f32 v[80:81], v[80:81], v[234:235] op_sel_hi:[1,0]\n"
"v_pk_add_f32 v[82:83], v[82:83], v[234:235] op_sel_hi:[1,0]\n"
"v_rcp_f32 v72, v72\n"
"v_rcp_f32 v73, v73\n"
"v_rcp_f32 v74, v74\n"
"v_rcp_f32 v75, v75\n"
"v_rcp_f32 v80, v80\n"
"v_rcp_f32 v81, v81\n"
"v_rcp_f32 v82, v82\n"
"v_rcp_f32 v83, v83\n"
"v_pk_fma_f32 v[52:53], v[52:53], v[142:143], v[142:143] op_sel:[0,1,0] op_sel_hi:[1,1,0]\n"
"v_pk_fma_f32 v[54:55], v[54:55], v[142:143], v[142:143] op_sel:[0,1,0] op_sel_hi:[1,1,0]\n"
"v_fmac_f32_dpp v112, v48, v144 row_shr:1 row_mask:0xf bank_mask:0xf\n"
"v_fmac_f32_dpp v113, v49, v145 row_shr:1 row_mask:0xf bank_mask:0xf\n"
"v_fmac_f32_dpp v114, v50, v146 row_shr:1 row_mask:0xf bank_mask:0xf\n"
"v_fmac_f32_dpp v115, v51, v147 row_shr:1 row_mask:0xf bank_mask:0xf\n"
"v_pk_fma_f32 v[64:65], v[64:65], v[142:143], v[142:143] op_sel:[0,1,0] op_sel_hi:[1,1,0]\n"
"v_pk_fma_f32 v[66:67], v[66:67], v[142:143], v[142:143] op_sel:[0,1,0] op_sel_hi:[1,1,0]\n"
"v_fmac_f32_dpp v120, v60, v148 row_shr:1 row_mask:0xf bank_mask:0xf\n"
"v_fmac_f32_dpp v121, v61, v149 row_shr:1 row_mask:0xf bank_mask:0xf\n"
"global_load_dwordx4 v[216:219], v238, s[70:71] sc1\n"
"global_load_dwordx4 v[220:223], v238, s[70:71] offset:1024 sc1\n"
"global_load_dwordx2 v[224:225], v252, s[80:81] offset:2048 sc1\n"
"v_fmac_f32_dpp v122, v62, v150 row_shr:1 row_mask:0xf bank_mask:0xf\n"
"v_fmac_f32_dpp v123, v63, v151 row_shr:1 row_mask:0xf bank_mask:0xf\n"
"v_cvt_pk_bf16_f32 v178, v212, v213\n"
"v_cvt_pk_bf16_f32 v179, v214, v215\n"
"v_pk_mul_f32 v[180:181], v[180:181], v[72:73]\n"
"v_pk_mul_f32 v[182:183], v[182:183], v[74:75]\n"
"v_pk_mul_f32 v[188:189], v[188:189], v[80:81]\n"
"v_pk_mul_f32 v[190:191], v[190:191], v[82:83]\n"
"v_fmac_f32_dpp v112, v52, v144 row_shl:15 row_mask:0xf bank_mask:0xf\n"
"v_fmac_f32_dpp v113, v53, v145 row_shl:15 row_mask:0xf bank_mask:0xf\n"
"v_fmac_f32_dpp v114, v54, v146 row_shl:15 row_mask:0xf bank_mask:0xf\n"
"v_fmac_f32_dpp v115, v55, v147 row_shl:15 row_mask:0xf bank_mask:0xf\n"
"v_fmac_f32_dpp v120, v64, v148 row_shl:15 row_mask:0xf bank_mask:0xf\n"
"v_fmac_f32_dpp v121, v65, v149 row_shl:15 row_mask:0xf bank_mask:0xf\n"
"v_fmac_f32_dpp v122, v66, v150 row_shl:15 row_mask:0xf bank_mask:0xf\n"
"v_fmac_f32_dpp v123, v67, v151 row_shl:15 row_mask:0xf bank_mask:0xf\n"
"v_pk_mul_f32 v[76:77], v[184:185], v[184:185]\n"
"v_pk_mul_f32 v[78:79], v[186:187], v[186:187]\n"
"v_pk_mul_f32 v[84:85], v[192:193], v[192:193]\n"
"v_pk_mul_f32 v[86:87], v[194:195], v[194:195]\n"
"v_pk_mul_f32 v[112:113], v[112:113], v[180:181]\n"
"v_pk_mul_f32 v[114:115], v[114:115], v[182:183]\n"
"v_pk_mul_f32 v[120:121], v[120:121], v[188:189]\n"
"v_pk_mul_f32 v[122:123], v[122:123], v[190:191]\n"
"v_pk_fma_f32 v[76:77], v[76:77], s[92:93], v[236:237] op_sel_hi:[1,1,0]\n"
"v_pk_fma_f32 v[78:79], v[78:79], s[92:93], v[236:237] op_sel_hi:[1,1,0]\n"
"v_pk_fma_f32 v[84:85], v[84:85], s[92:93], v[236:237] op_sel_hi:[1,1,0]\n"
"v_pk_fma_f32 v[86:87], v[86:87], s[92:93], v[236:237] op_sel_hi:[1,1,0]\n"
"v_cvt_pk_bf16_f32 v2, v112, v113\n"
"v_cvt_pk_bf16_f32 v3, v114, v115\n"
"v_cvt_pk_bf16_f32 v4, v120, v121\n"
"v_cvt_pk_bf16_f32 v5, v122, v123\n"
"v_pk_mul_f32 v[76:77], v[76:77], v[184:185]\n"
"v_pk_mul_f32 v[78:79], v[78:79], v[186:187]\n"
"v_pk_mul_f32 v[84:85], v[84:85], v[192:193]\n"
"v_pk_mul_f32 v[86:87], v[86:87], v[194:195]\n"
"v_exp_f32 v76, v76\n"
"v_exp_f32 v77, v77\n"
"v_exp_f32 v78, v78\n"
"v_exp_f32 v79, v79\n"
"v_exp_f32 v84, v84\n"
"v_exp_f32 v85, v85\n"
"v_exp_f32 v86, v86\n"
"v_exp_f32 v87, v87\n"
"v_pk_mul_f32 v[116:117], v[48:49], v[152:153]\n"
"v_pk_mul_f32 v[118:119], v[50:51], v[154:155]\n"
"v_pk_mul_f32 v[124:125], v[60:61], v[156:157]\n"
"v_pk_mul_f32 v[126:127], v[62:63], v[158:159]\n"
"v_pk_fma_f32 v[116:117], v[44:45], v[144:145], v[116:117]\n"
"v_pk_fma_f32 v[118:119], v[46:47], v[146:147], v[118:119]\n"
"v_pk_fma_f32 v[124:125], v[56:57], v[148:149], v[124:125]\n"
"v_pk_fma_f32 v[126:127], v[58:59], v[150:151], v[126:127]\n"
"v_pk_add_f32 v[76:77], v[76:77], v[234:235] op_sel_hi:[1,0]\n"
"v_pk_add_f32 v[78:79], v[78:79], v[234:235] op_sel_hi:[1,0]\n"
"v_pk_add_f32 v[84:85], v[84:85], v[234:235] op_sel_hi:[1,0]\n"
"v_pk_add_f32 v[86:87], v[86:87], v[234:235] op_sel_hi:[1,0]\n"
"v_rcp_f32 v76, v76\n"
"v_rcp_f32 v77, v77\n"
"v_rcp_f32 v78, v78\n"
"v_rcp_f32 v79, v79\n"
"v_rcp_f32 v84, v84\n"
"v_rcp_f32 v85, v85\n"
"v_rcp_f32 v86, v86\n"
"v_rcp_f32 v87, v87\n"
"v_fmac_f32_dpp v116, v44, v160 row_shl:1 row_mask:0xf bank_mask:0xf\n"
"v_fmac_f32_dpp v117, v45, v161 row_shl:1 row_mask:0xf bank_mask:0xf\n"
"v_fmac_f32_dpp v118, v46, v162 row_shl:1 row_mask:0xf bank_mask:0xf\n"
"v_fmac_f32_dpp v119, v47, v163 row_shl:1 row_mask:0xf bank_mask:0xf\n"
"v_fmac_f32_dpp v124, v56, v164 row_shl:1 row_mask:0xf bank_mask:0xf\n"
"v_fmac_f32_dpp v125, v57, v165 row_shl:1 row_mask:0xf bank_mask:0xf\n"
"v_fmac_f32_dpp v126, v58, v166 row_shl:1 row_mask:0xf bank_mask:0xf\n"
"v_fmac_f32_dpp v127, v59, v167 row_shl:1 row_mask:0xf bank_mask:0xf\n"
"v_pk_mul_f32 v[184:185], v[184:185], v[76:77]\n"
"v_pk_mul_f32 v[186:187], v[186:187], v[78:79]\n"
"v_pk_mul_f32 v[192:193], v[192:193], v[84:85]\n"
"v_pk_mul_f32 v[194:195], v[194:195], v[86:87]\n"
"v_fmac_f32_dpp v116, v52, v160 row_shr:15 row_mask:0xf bank_mask:0xf\n"
"v_fmac_f32_dpp v117, v53, v161 row_shr:15 row_mask:0xf bank_mask:0xf\n"
"v_fmac_f32_dpp v118, v54, v162 row_shr:15 row_mask:0xf bank_mask:0xf\n"
"v_fmac_f32_dpp v119, v55, v163 row_shr:15 row_mask:0xf bank_mask:0xf\n"
"v_fmac_f32_dpp v124, v64, v164 row_shr:15 row_mask:0xf bank_mask:0xf\n"
"v_fmac_f32_dpp v125, v65, v165 row_shr:15 row_mask:0xf bank_mask:0xf\n"
"v_fmac_f32_dpp v126, v66, v166 row_shr:15 row_mask:0xf bank_mask:0xf\n"
"v_fmac_f32_dpp v127, v67, v167 row_shr:15 row_mask:0xf bank_mask:0xf\n"
"v_pk_mul_f32 v[116:117], v[116:117], v[184:185]\n"
"v_pk_mul_f32 v[118:119], v[118:119], v[186:187]\n"
"v_pk_mul_f32 v[124:125], v[124:125], v[192:193]\n"
"v_pk_mul_f32 v[126:127], v[126:127], v[194:195]\n"
"v_cvt_pk_bf16_f32 v14, v116, v117\n"
"v_cvt_pk_bf16_f32 v15, v118, v119\n"
"v_cvt_pk_bf16_f32 v16, v124, v125\n"
"v_cvt_pk_bf16_f32 v17, v126, v127\n"
"s_waitcnt vmcnt(20)\n"
"v_mfma_f32_16x16x32_bf16 v[196:199], a[198:201], v[24:27], 0\n"
"ds_write_b64 v241, v[174:175] offset:7680\n"
"v_mov_b64 v[34:35], 0\n"
"v_mfma_f32_16x16x32_bf16 v[200:203], a[196:199], v[24:27], 0\n"
"ds_write_b64 v241, v[176:177] offset:8192\n"
"v_add_u32 v6, 110592, v238\n"
"s_waitcnt vmcnt(19)\n"
"v_mfma_f32_16x16x32_bf16 v[204:207], a[198:201], v[28:31], 0\n"
"ds_write_b64 v241, v[178:179] offset:8704\n"
"v_add_u32 v7, 110592, v23\n"
"v_mfma_f32_16x16x32_bf16 v[208:211], a[196:199], v[28:31], 0\n"
"ds_write_b128 v12, v[2:5] offset:0\n"
"v_mov_b64 v[226:227], 0\n"
"s_waitcnt vmcnt(18)\n"
"v_mfma_f32_16x16x32_bf16 v[212:215], a[198:201], v[32:35], 0\n"
"v_accvgpr_write_b32 a58, 0\n"
"v_accvgpr_write_b32 a59, 0\n"
"v_add_u32 v8, s50, v6\n"
"v_add_u32 v9, s50, v7\n"
"ds_write_b128 v12, v[14:17] offset:4608\n"
"s_waitcnt lgkmcnt(0)\n"
"s_barrier\n"
"ds_read_b64 v[72:73], v242 offset:0\n"
"ds_read_b64 v[74:75], v242 offset:9216\n"
"ds_read_b64 v[80:81], v242 offset:512\n"
"ds_read_b64 v[82:83], v242 offset:9728\n"
"ds_read_b64 v[88:89], v242 offset:4608\n"
"ds_read_b64 v[90:91], v242 offset:13824\n"
"ds_read_b64 v[96:97], v242 offset:5120\n"
"ds_read_b64 v[98:99], v242 offset:14336\n"
"ds_read_b64 v[104:105], v246 offset:0\n"
"ds_read_b64 v[106:107], v246 offset:9216\n"
"ds_read_b64 v[76:77], v242 offset:18432\n"
"ds_read_b64 v[78:79], v242 offset:27648\n"
"ds_read_b64 v[84:85], v242 offset:18944\n"
"ds_read_b64 v[86:87], v242 offset:28160\n"
"ds_read_b64 v[92:93], v242 offset:23040\n"
"ds_read_b64 v[94:95], v242 offset:32256\n"
"ds_read_b64 v[100:101], v242 offset:23552\n"
"ds_read_b64 v[102:103], v242 offset:32768\n"
"ds_read_b64 v[108:109], v246 offset:18432\n"
"ds_read_b64 v[110:111], v246 offset:27648\n"
"L_in0_%=:\n"
"s_waitcnt vmcnt(0)\n"
"v_max3_u32 v1, v216, v217, v218\n"
"v_max3_u32 v18, v219, v220, v221\n"
"v_max3_u32 v1, v1, v222, v223\n"
"v_max3_u32 v18, v18, v224, v225\n"
"v_max_u32 v1, v1, v18\n"
"v_cmp_eq_u32 vcc, -1, v1\n"
"s_nop 1\n"
"s_cbranch_vccnz L_in2_%=\n"
"L_in1_%=:\n"
"v_mfma_f32_16x16x32_bf16 v[112:115], a[198:201], v[216:219], 0\n"
"s_and_b32 s51, s77, 1\n"
"s_mul_i32 s50, s51, 4608\n"
"s_add_u32 s50, s50, 140288\n"
"v_mfma_f32_16x16x32_bf16 v[116:119], a[196:199], v[216:219], 0\n"
"s_lshl_b32 s51, s51, 8\n"
"s_add_u32 s51, s51, 121344\n"
"ds_read_b128 a[0:3], v6 offset:0\n"
"s_waitcnt lgkmcnt(15)\n"
"v_mfma_f32_16x16x32_bf16 v[112:115], a[202:205], v[72:75], v[112:115]\n"
"ds_read_b128 a[20:23], v6 offset:4608\n"
"ds_read_b128 a[4:7], v6 offset:1024\n"
"s_waitcnt lgkmcnt(15)\n"
"v_mfma_f32_16x16x32_bf16 v[116:119], a[202:205], v[80:83], v[116:119]\n"
"ds_read_b128 a[24:27], v6 offset:5632\n"
"ds_read_b128 a[8:11], v6 offset:2048\n"
"s_waitcnt lgkmcnt(13)\n"
"v_mfma_f32_16x16x32_bf16 v[112:115], a[202:205], v[76:79], v[112:115]\n"
"ds_read_b128 a[28:31], v6 offset:6656\n"
"v_add_u32 v13, s50, v23\n"
"s_waitcnt lgkmcnt(12)\n"
"v_mfma_f32_16x16x32_bf16 v[116:119], a[202:205], v[84:87], v[116:119]\n"
"ds_read_b128 a[12:15], v6 offset:3072\n"
"v_lshl_add_u32 v17, v0, 2, s51\n"
"v_mfma_f32_16x16x32_bf16 v[120:123], a[198:201], v[220:223], 0\n"
"s_add_u32 s51, s51, 192\n"
"ds_read_b128 a[32:35], v6 offset:7680\n"
"v_cvt_pk_bf16_f32 v44, v112, v113\n"
"v_mfma_f32_16x16x32_bf16 v[124:127], a[196:199], v[220:223], 0\n"
"ds_read_b128 a[40:43], v8 offset:0\n"
"v_cvt_pk_bf16_f32 v45, v114, v115\n"
"v_mfma_f32_16x16x32_bf16 v[120:123], a[202:205], v[88:91], v[120:123]\n"
"ds_read_b128 a[44:47], v8 offset:1024\n"
"v_cvt_pk_bf16_f32 v46, v116, v117\n"
"v_mfma_f32_16x16x32_bf16 v[124:127], a[202:205], v[96:99], v[124:127]\n"
"ds_read_b128 a[48:51], v8 offset:2048\n"
"v_cvt_pk_bf16_f32 v47, v118, v119\n"
"s_waitcnt lgkmcnt(15)\n"
"v_mfma_f32_16x16x32_bf16 v[120:123], a[202:205], v[92:95], v[120:123]\n"
"ds_write_b128 v12, v[44:47] offset:25600\n"
"v_lshlrev_b32 v1, 2, v21\n"
"s_waitcnt lgkmcnt(14)\n"
"v_mfma_f32_16x16x32_bf16 v[124:127], a[202:205], v[100:103], v[124:127]\n"
"v_add_u32 v14, 121536, v1\n"
"v_add_u32 v15, s51, v1\n"
"v_mfma_f32_16x16x32_bf16 v[128:131], a[202:205], v[224:227], 0\n"
"s_nop 0\n"
"v_cvt_pk_bf16_f32 v48, v120, v121\n"
"v_mfma_f32_16x16x32_bf16 v[128:131], a[202:205], v[104:107], v[128:131]\n"
"ds_read_b128 a[52:55], v8 offset:3072\n"
"v_cvt_pk_bf16_f32 v49, v122, v123\n"
"s_waitcnt lgkmcnt(13)\n"
"v_mfma_f32_16x16x32_bf16 v[128:131], a[202:205], v[108:111], v[128:131]\n"
"ds_read_b64 a[16:17], v7 offset:4096\n"
"v_cvt_pk_bf16_f32 v50, v124, v125\n"
"v_cvt_pk_bf16_f32 v51, v126, v127\n"
"s_waitcnt lgkmcnt(13)\n"
"v_mfma_f32_16x16x32_bf16 v[196:199], a[76:79], a[0:3], v[196:199]\n"
"ds_write_b128 v12, v[48:51] offset:30208\n"
"v_mov_b64 v[154:155], 0\n"
"v_mfma_f32_16x16x32_bf16 v[200:203], a[96:99], a[0:3], v[200:203]\n"
"s_waitcnt lgkmcnt(13)\n"
"v_cvt_pk_bf16_f32 v52, v128, v129\n"
"v_cvt_pk_bf16_f32 v53, v130, v131\n"
"v_mfma_f32_16x16x32_bf16 v[204:207], a[76:79], a[20:23], v[204:207]\n"
"s_waitcnt lgkmcnt(12)\n"
"v_mov_b64 v[174:175], 0\n"
"v_mov_b64 v[194:195], 0\n"
"v_mfma_f32_16x16x32_bf16 v[196:199], a[80:83], a[4:7], v[196:199]\n"
"ds_write_b64 v13, v[52:53]\n"
"v_mfma_f32_16x16x32_bf16 v[200:203], a[100:103], a[4:7], v[200:203]\n"
"ds_write_b32 v17, v131\n"
"s_waitcnt lgkmcnt(13)\n"
"v_mfma_f32_16x16x32_bf16 v[204:207], a[80:83], a[24:27], v[204:207]\n"
"ds_read_b64 a[36:37], v7 offset:8704\n"
"s_waitcnt lgkmcnt(13)\n"
"v_mfma_f32_16x16x32_bf16 v[196:199], a[84:87], a[8:11], v[196:199]\n"
"ds_read_b64 a[56:57], v9 offset:4096\n"
"v_mfma_f32_16x16x32_bf16 v[200:203], a[104:107], a[8:11], v[200:203]\n"
"s_waitcnt lgkmcnt(13)\n"
"v_mfma_f32_16x16x32_bf16 v[204:207], a[84:87], a[28:31], v[204:207]\n"
"s_waitcnt lgkmcnt(12)\n"
"v_mfma_f32_16x16x32_bf16 v[196:199], a[88:91], a[12:15], v[196:199]\n"
"v_mfma_f32_16x16x32_bf16 v[200:203], a[108:111], a[12:15], v[200:203]\n"
"s_waitcnt lgkmcnt(11)\n"
"v_mfma_f32_16x16x32_bf16 v[204:207], a[88:91], a[32:35], v[204:207]\n"
"s_waitcnt lgkmcnt(5)\n"
"v_mfma_f32_16x16x32_bf16 v[196:199], a[92:95], a[16:19], v[196:199]\n"
"v_mfma_f32_16x16x32_bf16 v[200:203], a[112:115], a[16:19], v[200:203]\n"
"s_waitcnt lgkmcnt(1)\n"
"v_mfma_f32_16x16x32_bf16 v[204:207], a[92:95], a[36:39], v[204:207]\n"
"s_waitcnt lgkmcnt(0)\n"
"s_barrier\n"
"ds_read_b128 v[136:139], v6 offset:25600\n"
"ds_read_b128 v[156:159], v6 offset:30208\n"
"ds_read_b128 v[176:179], v8 offset:25600\n"
"ds_read_b128 v[140:143], v6 offset:26624\n"
"v_mfma_f32_16x16x32_bf16 v[208:211], a[96:99], a[20:23], v[208:211]\n"
"s_cmp_lt_u32 s77, 2\n"
"s_cselect_b32 s84, s84, 1152000\n"
"ds_read_b128 v[160:163], v6 offset:31232\n"
"v_mfma_f32_16x16x32_bf16 v[212:215], a[116:119], a[40:43], v[212:215]\n"
"ds_read_b128 v[180:183], v8 offset:26624\n"
"v_mfma_f32_16x16x32_bf16 v[208:211], a[100:103], a[24:27], v[208:211]\n"
"ds_read_b128 v[144:147], v6 offset:27648\n"
"v_mfma_f32_16x16x32_bf16 v[212:215], a[120:123], a[44:47], v[212:215]\n"
"ds_read_b128 v[164:167], v6 offset:32256\n"
"v_mfma_f32_16x16x32_bf16 v[208:211], a[104:107], a[28:31], v[208:211]\n"
"ds_read_b128 v[184:187], v8 offset:27648\n"
"v_mfma_f32_16x16x32_bf16 v[212:215], a[124:127], a[48:51], v[212:215]\n"
"ds_read_b128 v[148:151], v6 offset:28672\n"
"s_waitcnt lgkmcnt(9)\n"
"v_mfma_f32_16x16x32_bf16 v[72:75], a[136:139], v[136:139], 0\n"
"ds_read_b128 v[168:171], v6 offset:33280\n"
"v_mfma_f32_16x16x32_bf16 v[76:79], a[156:159], v[136:139], 0\n"
"ds_read_b128 v[188:191], v8 offset:28672\n"
"s_waitcnt lgkmcnt(10)\n"
"v_mfma_f32_16x16x32_bf16 v[80:83], a[136:139], v[156:159], 0\n"
"ds_read_b64 v[152:153], v7 offset:29696\n"
"v_mfma_f32_16x16x32_bf16 v[84:87], a[156:159], v[156:159], 0\n"
"ds_read_b64 v[172:173], v7 offset:34304\n"
"ds_read_b64 v[192:193], v9 offset:29696\n"
"s_waitcnt lgkmcnt(12)\n"
"v_mfma_f32_16x16x32_bf16 v[88:91], a[176:179], v[176:179], 0\n"
"ds_read_b32 v64, v14\n"
"ds_read_b32 v66, v14 offset:256\n"
"s_waitcnt lgkmcnt(13)\n"
"v_mfma_f32_16x16x32_bf16 v[72:75], a[140:143], v[140:143], v[72:75]\n"
"ds_read_b32 v68, v15\n"
"v_mfma_f32_16x16x32_bf16 v[76:79], a[160:163], v[140:143], v[76:79]\n"
"s_waitcnt lgkmcnt(13)\n"
"v_mfma_f32_16x16x32_bf16 v[80:83], a[140:143], v[160:163], v[80:83]\n"
"v_mfma_f32_16x16x32_bf16 v[84:87], a[160:163], v[160:163], v[84:87]\n"
"s_waitcnt lgkmcnt(12)\n"
"v_mfma_f32_16x16x32_bf16 v[88:91], a[180:183], v[180:183], v[88:91]\n"
"s_waitcnt lgkmcnt(11)\n"
"v_mfma_f32_16x16x32_bf16 v[72:75], a[144:147], v[144:147], v[72:75]\n"
"v_mfma_f32_16x16x32_bf16 v[76:79], a[164:167], v[144:147], v[76:79]\n"
"s_waitcnt lgkmcnt(2)\n"
"v_rcp_f32 v64, v64\n"
"v_mfma_f32_16x16x32_bf16 v[80:83], a[144:147], v[164:167], v[80:83]\n"
"s_waitcnt lgkmcnt(1)\n"
"v_rcp_f32 v66, v66\n"
"v_mfma_f32_16x16x32_bf16 v[84:87], a[164:167], v[164:167], v[84:87]\n"
"s_waitcnt lgkmcnt(0)\n"
"v_rcp_f32 v68, v68\n"
"v_mfma_f32_16x16x32_bf16 v[88:91], a[184:187], v[184:187], v[88:91]\n"
"v_mfma_f32_16x16x32_bf16 v[72:75], a[148:151], v[148:151], v[72:75]\n"
"v_mfma_f32_16x16x32_bf16 v[76:79], a[168:171], v[148:151], v[76:79]\n"
"v_mfma_f32_16x16x32_bf16 v[208:211], a[108:111], a[32:35], v[208:211]\n"
"v_mfma_f32_16x16x32_bf16 v[80:83], a[148:151], v[168:171], v[80:83]\n"
"v_mfma_f32_16x16x32_bf16 v[84:87], a[168:171], v[168:171], v[84:87]\n"
"v_mfma_f32_16x16x32_bf16 v[212:215], a[128:131], a[52:55], v[212:215]\n"
"v_mfma_f32_16x16x32_bf16 v[88:91], a[188:191], v[188:191], v[88:91]\n"
"v_mfma_f32_16x16x32_bf16 v[72:75], a[152:155], v[152:155], v[72:75]\n"
"v_mfma_f32_16x16x32_bf16 v[76:79], a[172:175], v[152:155], v[76:79]\n"
"v_mfma_f32_16x16x32_bf16 v[208:211], a[112:115], a[36:39], v[208:211]\n"
"s_nop 2\n"
"v_fmac_f32 v196, v72, v64\n"
"v_mfma_f32_16x16x32_bf16 v[80:83], a[152:155], v[172:175], v[80:83]\n"
"v_fmac_f32 v197, v73, v64\n"
"v_fmac_f32 v198, v74, v64\n"
"v_mfma_f32_16x16x32_bf16 v[84:87], a[172:175], v[172:175], v[84:87]\n"
"v_fmac_f32 v199, v75, v64\n"
"v_fmac_f32 v200, v76, v64\n"
"v_mfma_f32_16x16x32_bf16 v[212:215], a[132:135], a[56:59], v[212:215]\n"
"v_fmac_f32 v201, v77, v64\n"
"v_fmac_f32 v202, v78, v64\n"
"v_mfma_f32_16x16x32_bf16 v[88:91], a[192:195], v[192:195], v[88:91]\n"
"v_fmac_f32 v203, v79, v64\n"
"v_cvt_pk_bf16_f32 v112, v196, v197\n"
"v_cvt_pk_bf16_f32 v113, v198, v199\n"
"v_cvt_pk_bf16_f32 v114, v200, v201\n"
"v_cvt_pk_bf16_f32 v115, v202, v203\n"
"v_fmac_f32 v204, v80, v66\n"
"v_fmac_f32 v205, v81, v66\n"
"v_fmac_f32 v206, v82, v66\n"
"v_fmac_f32 v207, v83, v66\n"
"v_fmac_f32 v208, v84, v66\n"
"v_fmac_f32 v209, v85, v66\n"
"v_fmac_f32 v210, v86, v66\n"
"v_fmac_f32 v211, v87, v66\n"
"buffer_store_dwordx4 v[112:115], v254, s[60:63], s83 offen offset:0\n"
"v_cvt_pk_bf16_f32 v116, v204, v205\n"
"v_cvt_pk_bf16_f32 v117, v206, v207\n"
"v_cvt_pk_bf16_f32 v118, v208, v209\n"
"v_cvt_pk_bf16_f32 v119, v210, v211\n"
"v_fmac_f32 v212, v88, v68\n"
"v_fmac_f32 v213, v89, v68\n"
"v_fmac_f32 v214, v90, v68\n"
"v_fmac_f32 v215, v91, v68\n"
"buffer_store_dwordx4 v[116:119], v254, s[60:63], s83 offen offset:288\n"
"v_cvt_pk_bf16_f32 v120, v212, v213\n"
"v_cvt_pk_bf16_f32 v121, v214, v215\n"
"buffer_store_dwordx2 v[120:121], v255, s[60:63], s84 offen offset:256\n"
"s_endpgm\n"
"L_in2_%=:\n"
"s_sub_u32 s53, s53, 1\n"
"s_cbranch_scc1 L_in1_%=\n"
"global_load_dwordx4 v[216:219], v238, s[70:71] sc1\n"
"global_load_dwordx4 v[220:223], v238, s[70:71] offset:1024 sc1\n"
"global_load_dwordx2 v[224:225], v252, s[80:81] offset:2048 sc1\n"
"s_branch L_in0_%=\n"
"L_first1_%=:\n"
"v_mfma_f32_16x16x32_bf16 v[24:27], a[0:3], a[76:79], 0\n"
"global_load_dword v20, v19, s[66:67] sc1\n"
"v_mfma_f32_16x16x32_bf16 v[24:27], a[4:7], a[80:83], v[24:27]\n"
"s_add_u32 m0, s45, 0\n"
"ds_read_b64_tr_b16 a[40:41], v8 offset:0\n"
"v_mfma_f32_16x16x32_bf16 v[24:27], a[8:11], a[84:87], v[24:27]\n"
"buffer_load_dwordx4 v10, s[40:43], s44 offen offset:0 sc1 lds\n"
"v_mfma_f32_16x16x32_bf16 v[24:27], a[12:15], a[88:91], v[24:27]\n"
"ds_read_b64_tr_b16 a[42:43], v8 offset:512\n"
"v_mfma_f32_16x16x32_bf16 v[24:27], a[16:19], a[92:95], v[24:27]\n"
"ds_read_b64_tr_b16 a[44:45], v8 offset:1024\n"
"v_mfma_f32_16x16x32_bf16 v[28:31], a[20:23], a[76:79], 0\n"
"buffer_load_dwordx4 v11, s[40:43], s44 offen offset:1024 sc1 lds\n"
"v_mfma_f32_16x16x32_bf16 v[28:31], a[24:27], a[80:83], v[28:31]\n"
"ds_read_b64_tr_b16 a[46:47], v8 offset:1536\n"
"v_mfma_f32_16x16x32_bf16 v[28:31], a[28:31], a[84:87], v[28:31]\n"
"v_mfma_f32_16x16x32_bf16 v[28:31], a[32:35], a[88:91], v[28:31]\n"
"ds_read_b64_tr_b16 a[48:49], v8 offset:2048\n"
"v_mfma_f32_16x16x32_bf16 v[28:31], a[36:39], a[92:95], v[28:31]\n"
"buffer_load_dwordx4 v12, s[40:43], s44 offen offset:2048 sc1 lds\n"
"v_mfma_f32_16x16x32_bf16 v[32:35], a[0:3], a[96:99], 0\n"
"ds_read_b64_tr_b16 a[50:51], v8 offset:2560\n"
"v_mfma_f32_16x16x32_bf16 v[32:35], a[4:7], a[100:103], v[32:35]\n"
"v_exp_f32 v24, v24\n"
"v_mfma_f32_16x16x32_bf16 v[32:35], a[8:11], a[104:107], v[32:35]\n"
"v_exp_f32 v25, v25\n"
"v_mfma_f32_16x16x32_bf16 v[32:35], a[12:15], a[108:111], v[32:35]\n"
"buffer_load_dwordx4 v13, s[40:43], s44 offen offset:3072 sc1 lds\n"
"v_mfma_f32_16x16x32_bf16 v[32:35], a[16:19], a[112:115], v[32:35]\n"
"v_exp_f32 v26, v26\n"
"v_mfma_f32_16x16x32_bf16 v[36:39], a[20:23], a[96:99], 0\n"
"ds_read_b64_tr_b16 a[52:53], v8 offset:3072\n"
"v_cvt_pk_bf16_f32 v56, v24, v25\n"
"v_mfma_f32_16x16x32_bf16 v[36:39], a[24:27], a[100:103], v[36:39]\n"
"s_add_u32 m0, s45, 4096\n"
"v_exp_f32 v27, v27\n"
"v_mfma_f32_16x16x32_bf16 v[36:39], a[28:31], a[104:107], v[36:39]\n"
"buffer_load_dwordx4 v14, s[40:43], s44 offen offset:0 sc1 lds\n"
"v_mfma_f32_16x16x32_bf16 v[36:39], a[32:35], a[108:111], v[36:39]\n"
"v_exp_f32 v28, v28\n"
"v_mfma_f32_16x16x32_bf16 v[36:39], a[36:39], a[112:115], v[36:39]\n"
"ds_read_b64_tr_b16 a[54:55], v8 offset:3584\n"
"v_cvt_pk_bf16_f32 v57, v26, v27\n"
"v_mfma_f32_16x16x32_bf16 v[40:43], a[0:3], a[116:119], 0\n"
"buffer_load_dwordx4 v15, s[40:43], s44 offen offset:1024 sc1 lds\n"
"v_mfma_f32_16x16x32_bf16 v[40:43], a[4:7], a[120:123], v[40:43]\n"
"v_exp_f32 v29, v29\n"
"v_mfma_f32_16x16x32_bf16 v[40:43], a[8:11], a[124:127], v[40:43]\n"
"v_exp_f32 v30, v30\n"
"v_mfma_f32_16x16x32_bf16 v[40:43], a[12:15], a[128:131], v[40:43]\n"
"ds_read_b64_tr_b16 a[56:57], v8 offset:4096\n"
"v_cvt_pk_bf16_f32 v58, v28, v29\n"
"v_mfma_f32_16x16x32_bf16 v[40:43], a[16:19], a[132:135], v[40:43]\n"
"buffer_load_dwordx4 v16, s[40:43], s44 offen offset:2048 sc1 lds\n"
"v_mfma_f32_16x16x32_bf16 v[44:47], a[20:23], a[116:119], 0\n"
"v_exp_f32 v31, v31\n"
"v_mfma_f32_16x16x32_bf16 v[44:47], a[24:27], a[120:123], v[44:47]\n"
"v_exp_f32 v32, v32\n"
"v_mfma_f32_16x16x32_bf16 v[44:47], a[28:31], a[124:127], v[44:47]\n"
"ds_read_b64_tr_b16 a[58:59], v8 offset:4608\n"
"v_cvt_pk_bf16_f32 v59, v30, v31\n"
"v_mfma_f32_16x16x32_bf16 v[44:47], a[32:35], a[128:131], v[44:47]\n"
"buffer_load_dwordx4 v17, s[40:43], s44 offen offset:3072 sc1 lds\n"
"v_mfma_f32_16x16x32_bf16 v[44:47], a[36:39], a[132:135], v[44:47]\n"
"v_exp_f32 v33, v33\n"
"v_mfma_f32_16x16x32_bf16 v[48:51], a[0:3], a[136:139], 0\n"
"v_exp_f32 v34, v34\n"
"v_mfma_f32_16x16x32_bf16 v[48:51], a[4:7], a[140:143], v[48:51]\n"
"s_add_u32 m0, s45, 8192\n"
"ds_read_b64_tr_b16 a[60:61], v8 offset:5120\n"
"v_cvt_pk_bf16_f32 v60, v32, v33\n"
"v_mfma_f32_16x16x32_bf16 v[48:51], a[8:11], a[144:147], v[48:51]\n"
"buffer_load_dwordx4 v18, s[40:43], s44 offen offset:0 sc1 lds\n"
"v_mfma_f32_16x16x32_bf16 v[48:51], a[12:15], a[148:151], v[48:51]\n"
"s_add_u32 s44, s44, 9216\n"
"v_exp_f32 v35, v35\n"
"v_mfma_f32_16x16x32_bf16 v[48:51], a[16:19], a[152:155], v[48:51]\n"
"v_exp_f32 v36, v36\n"
"v_mfma_f32_16x16x32_bf16 v[52:55], a[20:23], a[136:139], 0\n"
"ds_read_b64_tr_b16 a[62:63], v8 offset:5632\n"
"v_cvt_pk_bf16_f32 v61, v34, v35\n"
"v_mfma_f32_16x16x32_bf16 v[52:55], a[24:27], a[140:143], v[52:55]\n"
"v_exp_f32 v37, v37\n"
"v_mfma_f32_16x16x32_bf16 v[52:55], a[28:31], a[144:147], v[52:55]\n"
"v_exp_f32 v38, v38\n"
"v_exp_f32 v40, v40\n"
"v_mfma_f32_16x16x32_bf16 v[52:55], a[32:35], a[148:151], v[52:55]\n"
"ds_read_b64_tr_b16 a[64:65], v8 offset:6144\n"
"v_cvt_pk_bf16_f32 v62, v36, v37\n"
"v_exp_f32 v41, v41\n"
"v_mfma_f32_16x16x32_bf16 v[52:55], a[36:39], a[152:155], v[52:55]\n"
"s_waitcnt vmcnt(9) lgkmcnt(3)\n"
"v_exp_f32 v39, v39\n"
"s_nop 0\n"
"v_cvt_pk_bf16_f32 v63, v38, v39\n"
"v_exp_f32 v42, v42\n"
"v_mfma_f32_16x16x32_bf16 v[72:75], a[40:43], v[56:59], 0\n"
"ds_read_b64_tr_b16 a[66:67], v8 offset:6656\n"
"v_cmp_eq_u32 vcc, s49, v20\n"
"v_cvt_pk_bf16_f32 v64, v40, v41\n"
"v_mfma_f32_16x16x32_bf16 v[108:111], a[40:43], v[60:63], 0\n"
"ds_read_b64_tr_b16 a[68:69], v8 offset:7168\n"
"ds_read_b64_tr_b16 a[70:71], v8 offset:7680\n"
"v_mfma_f32_16x16x32_bf16 v[76:79], a[44:47], v[56:59], 0\n"
"s_and_b32 s48, vcc_lo, vcc_hi\n"
"ds_read_b64_tr_b16 a[72:73], v9 offset:8192\n"
"ds_read_b64_tr_b16 a[74:75], v9 offset:8704\n"
"v_exp_f32 v43, v43\n"
"v_mfma_f32_16x16x32_bf16 v[112:115], a[44:47], v[60:63], 0\n"
"s_andn2_b32 s50, s47, s48\n"
"ds_read_b128 a[0:3], v2 offset:36864\n"
"ds_read_b128 a[4:7], v2 offset:38912\n"
"v_mfma_f32_16x16x32_bf16 v[80:83], a[48:51], v[56:59], 0\n"
"s_cbranch_scc1 L_slowf1_%=\n"
"L_bkf1_%=:\n"
"ds_read_b128 a[8:11], v2 offset:40960\n"
"ds_read_b128 a[12:15], v2 offset:43008\n"
"v_mfma_f32_16x16x32_bf16 v[116:119], a[48:51], v[60:63], 0\n"
"v_exp_f32 v44, v44\n"
"v_mfma_f32_16x16x32_bf16 v[84:87], a[52:55], v[56:59], 0\n"
"ds_read_b64 a[16:17], v3 offset:36864\n"
"v_cvt_pk_bf16_f32 v65, v42, v43\n"
"v_mfma_f32_16x16x32_bf16 v[120:123], a[52:55], v[60:63], 0\n"
"ds_read_b128 a[20:23], v2 offset:37376\n"
"v_exp_f32 v45, v45\n"
"v_mfma_f32_16x16x32_bf16 v[88:91], a[56:59], v[56:59], 0\n"
"v_exp_f32 v46, v46\n"
"v_mfma_f32_16x16x32_bf16 v[124:127], a[56:59], v[60:63], 0\n"
"s_waitcnt lgkmcnt(10)\n"
"ds_read_b128 a[24:27], v2 offset:39424\n"
"v_cvt_pk_bf16_f32 v66, v44, v45\n"
"v_mfma_f32_16x16x32_bf16 v[92:95], a[60:63], v[56:59], 0\n"
"ds_read_b128 a[28:31], v2 offset:41472\n"
"v_exp_f32 v47, v47\n"
"v_mfma_f32_16x16x32_bf16 v[128:131], a[60:63], v[60:63], 0\n"
"v_exp_f32 v48, v48\n"
"v_mfma_f32_16x16x32_bf16 v[96:99], a[64:67], v[56:59], 0\n"
"ds_read_b128 a[32:35], v2 offset:43520\n"
"v_cvt_pk_bf16_f32 v67, v46, v47\n"
"v_mfma_f32_16x16x32_bf16 v[132:135], a[64:67], v[60:63], 0\n"
"s_waitcnt lgkmcnt(5)\n"
"ds_read_b64 a[36:37], v3 offset:37376\n"
"v_mfma_f32_16x16x32_bf16 v[100:103], a[68:71], v[56:59], 0\n"
"v_exp_f32 v49, v49\n"
"v_mfma_f32_16x16x32_bf16 v[136:139], a[68:71], v[60:63], 0\n"
"v_mfma_f32_16x16x32_bf16 v[104:107], a[72:75], v[56:59], 0\n"
"v_mfma_f32_16x16x32_bf16 v[140:143], a[72:75], v[60:63], 0\n"
"s_waitcnt lgkmcnt(0)\n"
"s_branch L_loop2_%=\n"
"L_slowf0_%=:\n"
"s_andn2_b32 s50, s47, s48\n"
"s_cmp_eq_u32 s50, 0\n"
"s_cbranch_scc1 L_slf0ok_%=\n"
"L_slf0sp_%=:\n"
"global_load_dword v20, v19, s[66:67] sc1\n"
"s_waitcnt vmcnt(0)\n"
"v_cmp_eq_u32 vcc, s49, v20\n"
"s_nop 1\n"
"s_and_b32 s48, vcc_lo, vcc_hi\n"
"s_sub_u32 s53, s53, 1\n"
"s_cselect_b32 s50, -1, 0\n"
"s_or_b32 s48, s48, s50\n"
"s_max_i32 s53, s53, 0\n"
"s_andn2_b32 s50, s47, s48\n"
"s_cmp_eq_u32 s50, 0\n"
"s_cbranch_scc0 L_slf0sp_%=\n"
"L_slf0ok_%=:\n"
"s_branch L_bkf0_%=\n"
"L_slowf1_%=:\n"
"s_andn2_b32 s50, s47, s48\n"
"s_cmp_eq_u32 s50, 0\n"
"s_cbranch_scc1 L_slf1ok_%=\n"
"L_slf1sp_%=:\n"
"global_load_dword v20, v19, s[66:67] sc1\n"
"s_waitcnt vmcnt(0)\n"
"v_cmp_eq_u32 vcc, s49, v20\n"
"s_nop 1\n"
"s_and_b32 s48, vcc_lo, vcc_hi\n"
"s_sub_u32 s53, s53, 1\n"
"s_cselect_b32 s50, -1, 0\n"
"s_or_b32 s48, s48, s50\n"
"s_max_i32 s53, s53, 0\n"
"s_andn2_b32 s50, s47, s48\n"
"s_cmp_eq_u32 s50, 0\n"
"s_cbranch_scc0 L_slf1sp_%=\n"
"L_slf1ok_%=:\n"
"s_branch L_bkf1_%=\n"
"L_wf_%=:\n"
"s_waitcnt vmcnt(0)\n"
"global_load_dword v20, v19, s[66:67] sc1\n"
"s_waitcnt vmcnt(0)\n"
"v_cmp_eq_u32 vcc, s49, v20\n"
"s_nop 1\n"
"s_and_b32 s48, vcc_lo, vcc_hi\n"
"s_lshr_b32 s50, s48, 17\n"
"s_andn2_b32 s50, 15, s50\n"
"s_cbranch_scc1 L_wfr_%=\n"
"s_lshr_b32 s50, %1, 4\n"
"s_mul_i32 s50, s50, 6144\n"
"v_lshlrev_b32 v222, 7, v0\n"
"v_min_u32 v222, 6016, v222\n"
"buffer_load_dword v223, v222, s[56:59], s50 offen sc1\n"
"s_branch L_wfr_%=\n"
"L_phs_%=:\n"
"s_lshr_b32 s50, s48, 17\n"
"s_andn2_b32 s50, 15, s50\n"
"s_cmp_eq_u32 s50, 0\n"
"s_cbranch_scc1 L_phok_%=\n"
"L_phsp_%=:\n"
"global_load_dword v20, v19, s[66:67] sc1\n"
"s_waitcnt vmcnt(0)\n"
"v_cmp_eq_u32 vcc, s49, v20\n"
"s_nop 1\n"
"s_and_b32 s48, vcc_lo, vcc_hi\n"
"s_sub_u32 s53, s53, 1\n"
"s_cselect_b32 s50, -1, 0\n"
"s_or_b32 s48, s48, s50\n"
"s_max_i32 s53, s53, 0\n"
"s_lshr_b32 s50, s48, 17\n"
"s_andn2_b32 s50, 15, s50\n"
"s_cmp_eq_u32 s50, 0\n"
"s_cbranch_scc0 L_phsp_%=\n"
"L_phok_%=:\n"
"s_branch L_phr_%=\n"
"L_fb_%=:\n"
"v_add_u32 v138, 136192, v10\n"
"v_add_u32 v139, 136192, v23\n"
"ds_read_b128 a[76:79], v138 offset:0\n"
"ds_read_b128 a[80:83], v138 offset:1024\n"
"ds_read_b128 a[84:87], v138 offset:2048\n"
"ds_read_b128 a[88:91], v138 offset:3072\n"
"ds_read_b64 a[92:93], v139 offset:4096\n"
"ds_read_b128 a[96:99], v138 offset:4608\n"
"ds_read_b128 a[100:103], v138 offset:5632\n"
"ds_read_b128 a[104:107], v138 offset:6656\n"
"ds_read_b128 a[108:111], v138 offset:7680\n"
"ds_read_b64 a[112:113], v139 offset:8704\n"
"ds_read_b128 a[116:119], v138 offset:9216\n"
"ds_read_b128 a[120:123], v138 offset:10240\n"
"ds_read_b128 a[124:127], v138 offset:11264\n"
"ds_read_b128 a[128:131], v138 offset:12288\n"
"ds_read_b64 a[132:133], v139 offset:13312\n"
"ds_read_b128 a[136:139], v138 offset:13824\n"
"ds_read_b128 a[140:143], v138 offset:14848\n"
"ds_read_b128 a[144:147], v138 offset:15872\n"
"ds_read_b128 a[148:151], v138 offset:16896\n"
"ds_read_b64 a[152:153], v139 offset:17920\n"
"s_waitcnt vmcnt(0) lgkmcnt(0)\n"
"s_mul_i32 s51, s74, 16128\n"
"v_add_u32 v64, 64, v0\n"
"v_min_u32 v64, 124, v64\n"
"v_lshl_add_u32 v64, v64, 7, s51\n"
"v_lshl_add_u32 v65, v0, 7, s51\n"
"L_fbsp_%=:\n"
"global_load_dword v66, v64, s[66:67] sc1\n"
"global_load_dword v67, v65, s[66:67] sc1\n"
"global_load_dword v68, v64, s[66:67] offset:64 sc1\n"
"global_load_dword v69, v65, s[66:67] offset:64 sc1\n"
"s_waitcnt vmcnt(0)\n"
"v_xor_b32 v66, s49, v66\n"
"v_xor_b32 v67, s49, v67\n"
"v_xor_b32 v68, s49, v68\n"
"v_xor_b32 v69, s49, v69\n"
"v_or3_b32 v66, v66, v67, v68\n"
"v_or_b32 v66, v66, v69\n"
"v_cmp_eq_u32 vcc, 0, v66\n"
"s_nop 1\n"
"s_cmp_eq_u64 vcc, -1\n"
"s_cbranch_scc1 L_fbgo_%=\n"
"s_sub_u32 s53, s53, 1\n"
"s_cbranch_scc0 L_fbsp_%=\n"
"s_mov_b32 s53, 0\n"
"L_fbgo_%=:\n"
"s_mul_i32 s51, s74, 1161216\n"
"s_add_u32 s16, s64, s51\n"
"s_addc_u32 s17, s65, 0\n"
"s_mov_b32 s16, s16\n"
"s_and_b32 s17, s17, 0xffff\n"
"s_mov_b32 s18, 1152000\n"
"s_mov_b32 s19, 0x20000\n"
"v_subrev_u32 v64, s45, v2\n"
"v_subrev_u32 v65, s45, v3\n"
"v_add_u32 v66, 0x1000, v64\n"
"s_mul_i32 s51, s77, 9216\n"
"v_mov_b32 v68, 0xff800000\n"
"v_mov_b32 v69, 0xff800000\n"
"v_mov_b32 v70, 0xff800000\n"
"v_mov_b32 v71, 0xff800000\n"
"L_fblp_%=:\n"
"buffer_load_dwordx4 a[0:3], v64, s[16:19], s51 offen offset:0 sc1\n"
"buffer_load_dwordx4 a[4:7], v64, s[16:19], s51 offen offset:2048 sc1\n"
"buffer_load_dwordx4 a[8:11], v66, s[16:19], s51 offen offset:0 sc1\n"
"buffer_load_dwordx4 a[12:15], v66, s[16:19], s51 offen offset:2048 sc1\n"
"buffer_load_dwordx2 a[16:17], v65, s[16:19], s51 offen offset:0 sc1\n"
"buffer_load_dwordx4 a[20:23], v64, s[16:19], s51 offen offset:512 sc1\n"
"buffer_load_dwordx4 a[24:27], v64, s[16:19], s51 offen offset:2560 sc1\n"
"buffer_load_dwordx4 a[28:31], v66, s[16:19], s51 offen offset:512 sc1\n"
"buffer_load_dwordx4 a[32:35], v66, s[16:19], s51 offen offset:2560 sc1\n"
"buffer_load_dwordx2 a[36:37], v65, s[16:19], s51 offen offset:512 sc1\n"
"s_waitcnt vmcnt(0)\n"
"v_mfma_f32_16x16x32_bf16 v[24:27], a[0:3], a[76:79], 0\n"
"v_mfma_f32_16x16x32_bf16 v[24:27], a[4:7], a[80:83], v[24:27]\n"
"v_mfma_f32_16x16x32_bf16 v[24:27], a[8:11], a[84:87], v[24:27]\n"
"v_mfma_f32_16x16x32_bf16 v[24:27], a[12:15], a[88:91], v[24:27]\n"
"v_mfma_f32_16x16x32_bf16 v[24:27], a[16:19], a[92:95], v[24:27]\n"
"v_mfma_f32_16x16x32_bf16 v[28:31], a[20:23], a[76:79], 0\n"
"v_mfma_f32_16x16x32_bf16 v[28:31], a[24:27], a[80:83], v[28:31]\n"
"v_mfma_f32_16x16x32_bf16 v[28:31], a[28:31], a[84:87], v[28:31]\n"
"v_mfma_f32_16x16x32_bf16 v[28:31], a[32:35], a[88:91], v[28:31]\n"
"v_mfma_f32_16x16x32_bf16 v[28:31], a[36:39], a[92:95], v[28:31]\n"
"v_mfma_f32_16x16x32_bf16 v[32:35], a[0:3], a[96:99], 0\n"
"v_mfma_f32_16x16x32_bf16 v[32:35], a[4:7], a[100:103], v[32:35]\n"
"v_mfma_f32_16x16x32_bf16 v[32:35], a[8:11], a[104:107], v[32:35]\n"
"v_mfma_f32_16x16x32_bf16 v[32:35], a[12:15], a[108:111], v[32:35]\n"
"v_mfma_f32_16x16x32_bf16 v[32:35], a[16:19], a[112:115], v[32:35]\n"
"v_mfma_f32_16x16x32_bf16 v[36:39], a[20:23], a[96:99], 0\n"
"v_mfma_f32_16x16x32_bf16 v[36:39], a[24:27], a[100:103], v[36:39]\n"
"v_mfma_f32_16x16x32_bf16 v[36:39], a[28:31], a[104:107], v[36:39]\n"
"v_mfma_f32_16x16x32_bf16 v[36:39], a[32:35], a[108:111], v[36:39]\n"
"v_mfma_f32_16x16x32_bf16 v[36:39], a[36:39], a[112:115], v[36:39]\n"
"v_mfma_f32_16x16x32_bf16 v[40:43], a[0:3], a[116:119], 0\n"
"v_mfma_f32_16x16x32_bf16 v[40:43], a[4:7], a[120:123], v[40:43]\n"
"v_mfma_f32_16x16x32_bf16 v[40:43], a[8:11], a[124:127], v[40:43]\n"
"v_mfma_f32_16x16x32_bf16 v[40:43], a[12:15], a[128:131], v[40:43]\n"
"v_mfma_f32_16x16x32_bf16 v[40:43], a[16:19], a[132:135], v[40:43]\n"
"v_mfma_f32_16x16x32_bf16 v[44:47], a[20:23], a[116:119], 0\n"
"v_mfma_f32_16x16x32_bf16 v[44:47], a[24:27], a[120:123], v[44:47]\n"
"v_mfma_f32_16x16x32_bf16 v[44:47], a[28:31], a[124:127], v[44:47]\n"
"v_mfma_f32_16x16x32_bf16 v[44:47], a[32:35], a[128:131], v[44:47]\n"
"v_mfma_f32_16x16x32_bf16 v[44:47], a[36:39], a[132:135], v[44:47]\n"
"v_mfma_f32_16x16x32_bf16 v[48:51], a[0:3], a[136:139], 0\n"
"v_mfma_f32_16x16x32_bf16 v[48:51], a[4:7], a[140:143], v[48:51]\n"
"v_mfma_f32_16x16x32_bf16 v[48:51], a[8:11], a[144:147], v[48:51]\n"
"v_mfma_f32_16x16x32_bf16 v[48:51], a[12:15], a[148:151], v[48:51]\n"
"v_mfma_f32_16x16x32_bf16 v[48:51], a[16:19], a[152:155], v[48:51]\n"
"v_mfma_f32_16x16x32_bf16 v[52:55], a[20:23], a[136:139], 0\n"
"v_mfma_f32_16x16x32_bf16 v[52:55], a[24:27], a[140:143], v[52:55]\n"
"v_mfma_f32_16x16x32_bf16 v[52:55], a[28:31], a[144:147], v[52:55]\n"
"v_mfma_f32_16x16x32_bf16 v[52:55], a[32:35], a[148:151], v[52:55]\n"
"v_mfma_f32_16x16x32_bf16 v[52:55], a[36:39], a[152:155], v[52:55]\n"
"s_nop 7\n"
"v_max3_f32 v68, v68, v24, v25\n"
"v_max3_f32 v68, v68, v26, v27\n"
"v_max3_f32 v68, v68, v28, v29\n"
"v_max3_f32 v68, v68, v30, v31\n"
"v_max3_f32 v69, v69, v32, v33\n"
"v_max3_f32 v69, v69, v34, v35\n"
"v_max3_f32 v69, v69, v36, v37\n"
"v_max3_f32 v69, v69, v38, v39\n"
"v_max3_f32 v70, v70, v40, v41\n"
"v_max3_f32 v70, v70, v42, v43\n"
"v_max3_f32 v70, v70, v44, v45\n"
"v_max3_f32 v70, v70, v46, v47\n"
"v_max3_f32 v71, v71, v48, v49\n"
"v_max3_f32 v71, v71, v50, v51\n"
"v_max3_f32 v71, v71, v52, v53\n"
"v_max3_f32 v71, v71, v54, v55\n"
"s_add_u32 s51, s51, 36864\n"
"s_cmp_lt_u32 s51, 1152000\n"
"s_cbranch_scc1 L_fblp_%=\n"
"v_mov_b32 v64, v68\n"
"v_mov_b32 v65, v69\n"
"v_mov_b32 v66, v70\n"
"v_mov_b32 v67, v71\n"
"v_permlane16_swap_b32 v68, v64\n"
"v_permlane16_swap_b32 v69, v65\n"
"v_permlane16_swap_b32 v70, v66\n"
"v_permlane16_swap_b32 v71, v67\n"
"v_max_f32 v68, v68, v64\n"
"v_max_f32 v69, v69, v65\n"
"v_max_f32 v70, v70, v66\n"
"v_max_f32 v71, v71, v67\n"
"v_mov_b32 v64, v68\n"
"v_mov_b32 v65, v69\n"
"v_mov_b32 v66, v70\n"
"v_mov_b32 v67, v71\n"
"v_permlane32_swap_b32 v68, v64\n"
"v_permlane32_swap_b32 v69, v65\n"
"v_permlane32_swap_b32 v70, v66\n"
"v_permlane32_swap_b32 v71, v67\n"
"v_max_f32 v68, v68, v64\n"
"v_max_f32 v69, v69, v65\n"
"v_max_f32 v70, v70, v66\n"
"v_max_f32 v71, v71, v67\n"
"s_lshl_b32 s50, s77, 8\n"
"s_add_u32 s50, s50, 110592\n"
"v_lshl_add_u32 v64, v21, 4, s50\n"
"ds_write_b128 v64, v[68:71]\n"
"s_waitcnt lgkmcnt(0)\n"
"s_barrier\n"
"v_lshlrev_b32 v64, 4, v21\n"
"v_add_u32 v64, 110592, v64\n"
"ds_read_b128 v[24:27], v64 offset:0\n"
"ds_read_b128 v[28:31], v64 offset:256\n"
"ds_read_b128 v[32:35], v64 offset:512\n"
"ds_read_b128 v[36:39], v64 offset:768\n"
"s_waitcnt lgkmcnt(0)\n"
"v_cmp_gt_u32 vcc, 16, v0\n"
"s_mov_b32 s51, 0xffff0000\n"
"v_max3_f32 v64, v24, v28, v32\n"
"v_max_f32 v64, v64, v36\n"
"v_sub_f32 v64, 0, v64\n"
"v_and_b32 v65, s51, v64\n"
"v_sub_f32 v64, v64, v65\n"
"v_and_b32 v66, s51, v64\n"
"v_sub_f32 v64, v64, v66\n"
"v_lshrrev_b32 v64, 16, v64\n"
"v_lshrrev_b32 v65, 16, v65\n"
"v_or_b32 v65, v65, v66\n"
"v_cndmask_b32 v65, 0, v65, vcc\n"
"v_cndmask_b32 v64, 0, v64, vcc\n"
"v_accvgpr_write_b32 a94, v65\n"
"v_accvgpr_write_b32 a95, v64\n"
"v_max3_f32 v64, v25, v29, v33\n"
"v_max_f32 v64, v64, v37\n"
"v_sub_f32 v64, 0, v64\n"
"v_and_b32 v65, s51, v64\n"
"v_sub_f32 v64, v64, v65\n"
"v_and_b32 v66, s51, v64\n"
"v_sub_f32 v64, v64, v66\n"
"v_lshrrev_b32 v64, 16, v64\n"
"v_lshrrev_b32 v65, 16, v65\n"
"v_or_b32 v65, v65, v66\n"
"v_cndmask_b32 v65, 0, v65, vcc\n"
"v_cndmask_b32 v64, 0, v64, vcc\n"
"v_accvgpr_write_b32 a114, v65\n"
"v_accvgpr_write_b32 a115, v64\n"
"v_max3_f32 v64, v26, v30, v34\n"
"v_max_f32 v64, v64, v38\n"
"v_sub_f32 v64, 0, v64\n"
"v_and_b32 v65, s51, v64\n"
"v_sub_f32 v64, v64, v65\n"
"v_and_b32 v66, s51, v64\n"
"v_sub_f32 v64, v64, v66\n"
"v_lshrrev_b32 v64, 16, v64\n"
"v_lshrrev_b32 v65, 16, v65\n"
"v_or_b32 v65, v65, v66\n"
"v_cndmask_b32 v65, 0, v65, vcc\n"
"v_cndmask_b32 v64, 0, v64, vcc\n"
"v_accvgpr_write_b32 a134, v65\n"
"v_accvgpr_write_b32 a135, v64\n"
"v_max3_f32 v64, v27, v31, v35\n"
"v_max_f32 v64, v64, v39\n"
"v_sub_f32 v64, 0, v64\n"
"v_and_b32 v65, s51, v64\n"
"v_sub_f32 v64, v64, v65\n"
"v_and_b32 v66, s51, v64\n"
"v_sub_f32 v64, v64, v66\n"
"v_lshrrev_b32 v64, 16, v64\n"
"v_lshrrev_b32 v65, 16, v65\n"
"v_or_b32 v65, v65, v66\n"
"v_cndmask_b32 v65, 0, v65, vcc\n"
"v_cndmask_b32 v64, 0, v64, vcc\n"
"v_accvgpr_write_b32 a154, v65\n"
"v_accvgpr_write_b32 a155, v64\n"
"v_mov_b32 v64, 0x3f803f80\n"
"v_mov_b32 v65, 0x3f80\n"
"v_cndmask_b32 v64, 0, v64, vcc\n"
"v_cndmask_b32 v65, 0, v65, vcc\n"
"v_accvgpr_write_b32 a18, v64\n"
"v_accvgpr_write_b32 a19, v65\n"
"v_accvgpr_write_b32 a38, v64\n"
"v_accvgpr_write_b32 a39, v65\n"
"s_barrier\n"
"s_branch L_fbret_%=\n"
"L_end_%=:\n"
  :: "s"(__builtin_amdgcn_kernarg_segment_ptr()), "s"(b), "s"(wave)
  : "v0", "v1", "v2", "v3", "v4", "v5", "v6", "v7", "v8", "v9", "v10", "v11", "v12", "v13", "v14", "v15", "v16", "v17", "v18", "v19", "v20", "v21", "v22", "v23", "v24", "v25", "v26", "v27", "v28", "v29", "v30", "v31", "v32", "v33", "v34", "v35", "v36", "v37", "v38", "v39", "v40", "v41", "v42", "v43", "v44", "v45", "v46", "v47", "v48", "v49", "v50", "v51", "v52", "v53", "v54", "v55", "v56", "v57", "v58", "v59", "v60", "v61", "v62", "v63", "v64", "v65", "v66", "v67", "v68", "v69", "v70", "v71", "v72", "v73", "v74", "v75", "v76", "v77", "v78", "v79", "v80", "v81", "v82", "v83", "v84", "v85", "v86", "v87", "v88", "v89", "v90", "v91", "v92", "v93", "v94", "v95", "v96", "v97", "v98", "v99", "v100", "v101", "v102", "v103", "v104", "v105", "v106", "v107", "v108", "v109", "v110", "v111", "v112", "v113", "v114", "v115", "v116", "v117", "v118", "v119", "v120", "v121", "v122", "v123", "v124", "v125", "v126", "v127", "v128", "v129", "v130", "v131", "v132", "v133", "v134", "v135", "v136", "v137", "v138", "v139", "v140", "v141", "v142", "v143", "v144", "v145", "v146", "v147", "v148", "v149", "v150", "v151", "v152", "v153", "v154", "v155", "v156", "v157", "v158", "v159", "v160", "v161", "v162", "v163", "v164", "v165", "v166", "v167", "v168", "v169", "v170", "v171", "v172", "v173", "v174", "v175", "v176", "v177", "v178", "v179", "v180", "v181", "v182", "v183", "v184", "v185", "v186", "v187", "v188", "v189", "v190", "v191", "v192", "v193", "v194", "v195", "v196", "v197", "v198", "v199", "v200", "v201", "v202", "v203", "v204", "v205", "v206", "v207", "v208", "v209", "v210", "v211", "v212", "v213", "v214", "v215", "v216", "v217", "v218", "v219", "v220", "v221", "v222", "v223", "v224", "v225", "v226", "v227", "v228", "v229", "v230", "v231", "v232", "v233", "v234", "v235", "v236", "v237", "v238", "v239", "v240", "v241", "v242", "v243", "v244", "v245", "v246", "v247", "v248", "v249", "v250", "v251", "v252", "v253", "v254", "v255", "a0", "a1", "a2", "a3", "a4", "a5", "a6", "a7", "a8", "a9", "a10", "a11", "a12", "a13", "a14", "a15", "a16", "a17", "a18", "a19", "a20", "a21", "a22", "a23", "a24", "a25", "a26", "a27", "a28", "a29", "a30", "a31", "a32", "a33", "a34", "a35", "a36", "a37", "a38", "a39", "a40", "a41", "a42", "a43", "a44", "a45", "a46", "a47", "a48", "a49", "a50", "a51", "a52", "a53", "a54", "a55", "a56", "a57", "a58", "a59", "a60", "a61", "a62", "a63", "a64", "a65", "a66", "a67", "a68", "a69", "a70", "a71", "a72", "a73", "a74", "a75", "a76", "a77", "a78", "a79", "a80", "a81", "a82", "a83", "a84", "a85", "a86", "a87", "a88", "a89", "a90", "a91", "a92", "a93", "a94", "a95", "a96", "a97", "a98", "a99", "a100", "a101", "a102", "a103", "a104", "a105", "a106", "a107", "a108", "a109", "a110", "a111", "a112", "a113", "a114", "a115", "a116", "a117", "a118", "a119", "a120", "a121", "a122", "a123", "a124", "a125", "a126", "a127", "a128", "a129", "a130", "a131", "a132", "a133", "a134", "a135", "a136", "a137", "a138", "a139", "a140", "a141", "a142", "a143", "a144", "a145", "a146", "a147", "a148", "a149", "a150", "a151", "a152", "a153", "a154", "a155", "a156", "a157", "a158", "a159", "a160", "a161", "a162", "a163", "a164", "a165", "a166", "a167", "a168", "a169", "a170", "a171", "a172", "a173", "a174", "a175", "a176", "a177", "a178", "a179", "a180", "a181", "a182", "a183", "a184", "a185", "a186", "a187", "a188", "a189", "a190", "a191", "a192", "a193", "a194", "a195", "a196", "a197", "a198", "a199", "a200", "a201", "a202", "a203", "a204", "a205", "a206", "a207", "a208", "a209", "a210", "a211", "a212", "a213", "a214", "a215", "a216", "a217", "a218", "a219", "a220", "a221", "a222", "a223", "a224", "a225", "a226", "a227", "a228", "a229", "a230", "a231", "a232", "a233", "a234", "a235", "a236", "a237", "a238", "a239", "a240", "a241", "a242", "a243", "a244", "a245", "a246", "a247", "a248", "a249", "a250", "a251", "a252", "a253", "a254", "a255", "s16", "s17", "s18", "s19", "s20", "s21", "s22", "s23", "s24", "s25", "s26", "s27", "s28", "s29", "s30", "s31", "s35", "s36", "s37", "s38", "s39", "s40", "s41", "s42", "s43", "s44", "s45", "s46", "s47", "s48", "s49", "s50", "s51", "s52", "s53", "s54", "s55", "s56", "s57", "s58", "s59", "s60", "s61", "s62", "s63", "s64", "s65", "s66", "s67", "s68", "s69", "s70", "s71", "s72", "s73", "s74", "s75", "s76", "s77", "s78", "s79", "s80", "s81", "s82", "s83", "s84", "s85", "s86", "s87", "s88", "s89", "s90", "s91", "s92", "s93", "s94", "s95", "s96", "s97", "s98", "s99", "vcc", "scc", "memory");
  __builtin_unreachable();
  }
  {
    const unsigned hid = (b >> 1) & 3;
    int lane;
    asm volatile("v_mbcnt_lo_u32_b32 %0, -1, 0\n\tv_mbcnt_hi_u32_b32 %0, -1, %0" : "=v"(lane));
    const int tid = wave * 64 + lane;
    char* ws; const u16 *wv, *wao, *wdn, *wm;
    asm volatile("s_load_dwordx2 %0, %5, 0x18\n\ts_load_dwordx2 %1, %5, 0x38\n\ts_load_dwordx2 %2, %5, 0x40\n\ts_load_dwordx2 %3, %5, 0x48\n\t"
                 "s_load_dwordx2 %4, %5, 0x50\n\ts_waitcnt lgkmcnt(0)"
                 : "=&s"(ws), "=&s"(wv), "=&s"(wao), "=&s"(wdn), "=&s"(wm) : "s"(__builtin_amdgcn_kernarg_segment_ptr()) : "memory");
    unsigned* flags = (unsigned*)(ws + WS_FLAGS);
    char *A = lds, *Bm = lds + 41472, *Cm = lds + 82944;
    f32x4 d[9];
    if (hid == 0) {
      stage(A, wdn, wave, lane); stage(Bm, wm + 144 * 144, wave, lane);
      asm volatile("s_waitcnt vmcnt(0)" ::: "memory");
      __syncthreads();
#pragma unroll 1
      for (int t = wave; t < 9; t += 4) {
        rows_mm<true, false>(A, load_b(Bm, out_cols(t, lane), lane), d, lane);
        put(ws + WS_WF, t, pack(d), lane);
      }
    } else {
      stage(A, wao, wave, lane); stage(Bm, wm, wave, lane); stage(Cm, wv, wave, lane);
      asm volatile("s_waitcnt vmcnt(0)" ::: "memory");
      __syncthreads();
      if (wave < 3) {
        const int t = 3 * (hid - 1) + wave, g = lane >> 4;
        rows_mm<true, false>(A, load_b(Bm, out_cols(t, lane), lane), d, lane);
        rows_mm<false, true>(Cm, pack(d), d, lane);
        float s = 0.f;
#pragma unroll
        for (int tr = 0; tr < 9; ++tr) s += (d[tr][0] + d[tr][1]) + (d[tr][2] + d[tr][3]);
        const float k = xg_sum((g == 3 ? d[8][3] : 0.f) - s * (1.f / 12.f)) * (1.f / 11.f);
#pragma unroll
        for (int tr = 0; tr < 9; ++tr) d[tr] += k;
        if (g == 3) d[8][3] = 0.f;
        put(ws + WS_WF + 46080, t, pack(d), lane);
      }
    }
    const unsigned ep = __hip_atomic_load((unsigned*)(ws + WS_G), __ATOMIC_RELAXED, __HIP_MEMORY_SCOPE_AGENT) + 1u;
    asm volatile("s_waitcnt vmcnt(0)" ::: "memory");
    __syncthreads();
    if (tid == 0) __hip_atomic_store(flags + (252 + hid) * 32, ep, __ATOMIC_RELAXED, __HIP_MEMORY_SCOPE_AGENT);
    if (hid == 2 && wave == 0) {
      for (int spin = 0; spin < (1 << 18); ++spin) {
        bool ok = true;
#pragma unroll
        for (int k = 0; k < 4; ++k) {
          const int f = 64 * k + lane;
          ok &= __hip_atomic_load(flags + f * 32, __ATOMIC_RELAXED, __HIP_MEMORY_SCOPE_AGENT) == ep;
          ok &= __hip_atomic_load(flags + (f < 252 ? f : 0) * 32 + 16, __ATOMIC_RELAXED, __HIP_MEMORY_SCOPE_AGENT) == ep;
        }
        if (__all(ok)) break;
        __builtin_amdgcn_s_sleep(8);
      }
      if (lane == 0) __hip_atomic_store((unsigned*)(ws + WS_G), ep, __ATOMIC_RELAXED, __HIP_MEMORY_SCOPE_AGENT);
    }
    return;
  }
}

extern "C" void kernel_launch(void* const* d_in, const int* in_sizes, int n_in, void* d_out, int out_size, void* d_ws, size_t ws_size,
                              hipStream_t stream) {
  (void)hipFuncSetAttribute((const void*)bf_fused, hipFuncAttributeMaxDynamicSharedMemorySize, 163840);
  hipLaunchKernelGGL(bf_fused, dim3(256), dim3(256), 163840, stream, (const u16*)d_in[0], (const u16*)d_in[1], (const u16*)d_in[2],
                     (char*)d_ws, (const u16*)d_in[5], (const u16*)d_in[6], (u16*)d_out, (const u16*)d_in[3], (const u16*)d_in[4],
                     (const u16*)d_in[7], (const u16*)d_in[8]);
}
```
